```python
import torch

def _softmax(x, axis=-1):
    return torch.softmax(x, dim=axis)

def _gelu(x):
    return torch.nn.functional.gelu(x, approximate='tanh')

def _silu(x):
    return torch.nn.functional.silu(x)

def _sigmoid(x):
    return torch.sigmoid(x)

def _layernorm(x, axis=-1, eps=1e-05):
    m = x.mean(dim=axis, keepdim=True)
    v = x.var(dim=axis, keepdim=True, unbiased=False)
    return (x - m) / torch.sqrt(v + eps)

def _scatter_add(tgt, idx, src, axis=0):
    return tgt.clone().index_add_(axis, idx, src)

def _segment_sum(src, seg, n):
    out = torch.zeros((n,) + src.shape[1:], dtype=src.dtype)
    return out.index_add_(0, seg, src)

def _pad(x, pw):
    flat = tuple((v for pair in reversed(pw) for v in pair))
    return torch.nn.functional.pad(x, flat)
kernel_name = 'adaLN_cond__sliding_local_dilated__glu_gated_geglu__prompt'

def setup_inputs():

    _w = lambda a: (lambda t: t.to(torch.bfloat16) if t.is_floating_point() else t)(torch.as_tensor(a))

    def _A_setup():
        B, S, D = (1, 2048, 512)
        return {'x': _w((lambda *s: torch.randn(*s, dtype=torch.bfloat16) * 0.05)(B, S, D)), 'wqkv': _w((lambda *s: torch.randn(*s, dtype=torch.bfloat16) * 0.05)(D, 3 * D)), 'wo': _w((lambda *s: torch.randn(*s, dtype=torch.bfloat16) * 0.05)(D, D))}
    _A_in = _A_setup()

    def _B_setup():
        B, S, Dm, Df = (1, 2048, 512, 1792)
        return {'x': _w((lambda *s: torch.randn(*s, dtype=torch.bfloat16) * 0.05)(B, S, Dm)), 'w_gate': _w((lambda *s: torch.randn(*s, dtype=torch.bfloat16) * 0.8)(Dm, Df) * Dm ** (-0.5)), 'w_up': _w((lambda *s: torch.randn(*s, dtype=torch.bfloat16) * 0.8)(Dm, Df) * Dm ** (-0.5)), 'w_down': _w((lambda *s: torch.randn(*s, dtype=torch.bfloat16) * 0.8)(Df, Dm) * Df ** (-0.5))}
    _B_in = _B_setup()
    return {'A_x': _A_in['x'], 'A_wqkv': _A_in['wqkv'], 'A_wo': _A_in['wo'], 'B_x': _B_in['x'], 'B_w_gate': _B_in['w_gate'], 'B_w_up': _B_in['w_up'], 'B_w_down': _B_in['w_down'], 'c': _w((lambda *s: torch.randn(*s, dtype=torch.bfloat16) * 0.2)(1, 512) * 8.0), 'w_mod': _w((lambda *s: torch.randn(*s, dtype=torch.bfloat16) * 0.2)(512, 6 * 512) * 512 ** (-0.5) * 8.0)}

def reference(A_x, A_wqkv, A_wo, B_x, B_w_gate, B_w_up, B_w_down, c, w_mod):

    def _A_ref(x, wqkv, wo):
        B, S, D = x.shape
        H = 32
        Dh = D // H
        W, dil = (8, 2)
        qkv = (lambda x, *d: x.permute(*d))((x @ wqkv).reshape(B, S, 3, H, Dh), 2, 0, 3, 1, 4)
        q, k, v = (qkv[0], qkv[1], qkv[2])
        scale = Dh ** (-0.5)
        pos = torch.arange(S)
        off = torch.arange(W) * dil
        idx = pos[:, None] - off[None, :]
        mask = idx >= 0
        idx_c = torch.where(mask, idx, 0).reshape(-1)
        kg = k[:, :, idx_c, :].reshape(B, H, S, W, Dh)
        vg = v[:, :, idx_c, :].reshape(B, H, S, W, Dh)
        logits = torch.einsum('bhsd,bhswd->bhsw', q, kg) * scale
        logits = torch.where(mask[None, None, :, :], logits, -1000000000.0)
        attn = _softmax(logits, axis=-1)
        o = torch.einsum('bhsw,bhswd->bhsd', attn, vg)
        return (lambda x, *d: x.permute(*d))(o, 0, 2, 1, 3).reshape(B, S, D) @ wo

    def _B_ref(x, w_gate, w_up, w_down):
        g = torch.einsum('bsd,df->bsf', x, w_gate)
        u = torch.einsum('bsd,df->bsf', x, w_up)
        h = _gelu(g) * u
        return torch.einsum('bsf,fd->bsd', h, w_down)
    x = A_x
    mod = (_silu(c) @ w_mod).reshape(c.shape[0], 6, 1, -1)
    sa, ca, ga, sb, cb, gb = (mod[:, 0].clone(), mod[:, 1].clone(), mod[:, 2].clone(), mod[:, 3].clone(), mod[:, 4].clone(), mod[:, 5].clone())
    h = _layernorm(x, axis=-1) * (1 + ca) + sa
    x = x + ga * _A_ref(h, A_wqkv, A_wo)
    h = _layernorm(x, axis=-1) * (1 + cb) + sb
    x = x + gb * _B_ref(h, B_w_gate, B_w_up, B_w_down)
    return x
```

```cpp
#include <hip/hip_runtime.h>
#include <hip/hip_bf16.h>
#include <stdint.h>

typedef unsigned short u16;
__device__ __forceinline__ float bf2f(u16 v){ return __uint_as_float(((unsigned)v)<<16); }
__device__ __forceinline__ u16 f2bf(float f){ unsigned u=__float_as_uint(f); u += 0x7FFF + ((u>>16)&1); return (u16)(u>>16); }
__device__ __forceinline__ float rbf(float f){ return bf2f(f2bf(f)); }

constexpr int S=2048, D=512, DF=1792, H=32, DH=16;

__global__ void k_mod(const u16* c, const u16* wmod, float* mod){
  int n = blockIdx.x*blockDim.x+threadIdx.x;
  if(n>=6*D) return;
  float acc=0.f;
  for(int k=0;k<D;k++){ float cv=bf2f(c[k]); float s=rbf(cv/(1.f+__expf(-cv))); acc+= s*bf2f(wmod[(size_t)k*6*D+n]); }
  mod[n]=rbf(acc);
}
__global__ void k_ln(const u16* x, const float* mod, int shift_off, int scale_off, u16* h){
  int row=blockIdx.x; int t=threadIdx.x;
  __shared__ float red[256];
  float a=bf2f(x[row*D+t]), b=bf2f(x[row*D+t+256]);
  red[t]=a+b; __syncthreads();
  for(int s=128;s>0;s>>=1){ if(t<s) red[t]+=red[t+s]; __syncthreads(); }
  float mean=red[0]/D; __syncthreads();
  float da=a-mean, db=b-mean;
  red[t]=da*da+db*db; __syncthreads();
  for(int s=128;s>0;s>>=1){ if(t<s) red[t]+=red[t+s]; __syncthreads(); }
  float var=red[0]/D;
  float m=rbf(mean), v=rbf(var);
  float den=rbf(sqrtf(rbf(v+1e-5f)));
  for(int i=0;i<2;i++){
    int d=t+i*256; float xv=i?b:a;
    float n=rbf(rbf(xv-m)/den);
    float sc=rbf(1.f+mod[scale_off+d]);
    float hv=rbf(rbf(n*sc)+mod[shift_off+d]);
    h[row*D+d]=f2bf(hv);
  }
}
__global__ void k_gemm(const u16* A, const u16* W, u16* C, int M, int N, int K){
  __shared__ float sA[16][17], sB[16][17];
  int tx=threadIdx.x, ty=threadIdx.y;
  int row=blockIdx.y*16+ty, col=blockIdx.x*16+tx;
  float acc=0.f;
  for(int k0=0;k0<K;k0+=16){
    sA[ty][tx]=bf2f(A[(size_t)row*K+k0+tx]);
    sB[ty][tx]=bf2f(W[(size_t)(k0+ty)*N+col]);
    __syncthreads();
    for(int k=0;k<16;k++) acc+=sA[ty][k]*sB[k][tx];
    __syncthreads();
  }
  C[(size_t)row*N+col]=f2bf(acc);
}
__global__ void k_attn(const u16* qkv, u16* o){
  int s=blockIdx.x; int h=threadIdx.x;
  if(h>=H) return;
  float q[DH];
  for(int d=0;d<DH;d++) q[d]=bf2f(qkv[(size_t)s*3*D + h*DH+d]);
  float lg[8]; float mx=-1e30f;
  for(int w=0;w<8;w++){
    int idx=s-2*w;
    float l;
    if(idx>=0){ float a=0; for(int d=0;d<DH;d++) a+=q[d]*bf2f(qkv[(size_t)idx*3*D + D + h*DH+d]); l=rbf(rbf(a)*0.25f);} else l=-1e9f;
    lg[w]=l; mx=fmaxf(mx,l);
  }
  float sum=0; for(int w=0;w<8;w++){ lg[w]=__expf(lg[w]-mx); sum+=lg[w]; }
  float ov[DH]; for(int d=0;d<DH;d++) ov[d]=0;
  for(int w=0;w<8;w++){
    int idx=s-2*w; if(idx<0) idx=0;
    float p=rbf(lg[w]/sum);
    for(int d=0;d<DH;d++) ov[d]+=p*bf2f(qkv[(size_t)idx*3*D + 2*D + h*DH+d]);
  }
  for(int d=0;d<DH;d++) o[(size_t)s*D+h*DH+d]=f2bf(ov[d]);
}
__global__ void k_res(const u16* x, const u16* y, const float* mod, int goff, u16* out){
  int i=blockIdx.x*blockDim.x+threadIdx.x; if(i>=S*D) return;
  int d=i%D;
  out[i]=f2bf(bf2f(x[i]) + rbf(mod[goff+d]*bf2f(y[i])));
}
__global__ void k_glu(const u16* g, const u16* u, u16* hh){
  int i=blockIdx.x*blockDim.x+threadIdx.x; if(i>=S*DF) return;
  float gv=bf2f(g[i]);
  float t=tanhf(0.7978845608028654f*(gv+0.044715f*gv*gv*gv));
  float ge=rbf(0.5f*gv*(1.f+t));
  hh[i]=f2bf(ge*bf2f(u[i]));
}

extern "C" void kernel_launch(void* const* d_in, const int* in_sizes, int n_in,
                              void* d_out, int out_size, void* d_ws, size_t ws_size,
                              hipStream_t stream) {
  const u16* x=(const u16*)d_in[0]; const u16* wqkv=(const u16*)d_in[1]; const u16* wo=(const u16*)d_in[2];
  const u16* wg=(const u16*)d_in[4]; const u16* wu=(const u16*)d_in[5]; const u16* wd=(const u16*)d_in[6];
  const u16* c=(const u16*)d_in[7]; const u16* wmod=(const u16*)d_in[8];
  char* ws=(char*)d_ws;
  float* mod=(float*)ws; ws+=16384;
  u16* h=(u16*)ws; ws+=S*D*2;
  u16* qkv=(u16*)ws; ws+=S*3*D*2;
  u16* o=(u16*)ws; ws+=S*D*2;
  u16* ao=(u16*)ws; ws+=S*D*2;
  u16* x1=(u16*)ws; ws+=S*D*2;
  u16* g=(u16*)ws; ws+=S*DF*2;
  u16* u=(u16*)ws; ws+=S*DF*2;
  u16* hh=(u16*)ws; ws+=S*DF*2;
  u16* dn=(u16*)ws; ws+=S*D*2;
  k_mod<<<12,256,0,stream>>>(c,wmod,mod);
  k_ln<<<S,256,0,stream>>>(x,mod,0,D,h);
  k_gemm<<<dim3(3*D/16,S/16),dim3(16,16),0,stream>>>(h,wqkv,qkv,S,3*D,D);
  k_attn<<<S,64,0,stream>>>(qkv,o);
  k_gemm<<<dim3(D/16,S/16),dim3(16,16),0,stream>>>(o,wo,ao,S,D,D);
  k_res<<<S*D/256,256,0,stream>>>(x,ao,mod,2*D,x1);
  k_ln<<<S,256,0,stream>>>(x1,mod,3*D,4*D,h);
  k_gemm<<<dim3(DF/16,S/16),dim3(16,16),0,stream>>>(h,wg,g,S,DF,D);
  k_gemm<<<dim3(DF/16,S/16),dim3(16,16),0,stream>>>(h,wu,u,S,DF,D);
  k_glu<<<S*DF/256,256,0,stream>>>(g,u,hh);
  k_gemm<<<dim3(D/16,S/16),dim3(16,16),0,stream>>>(hh,wd,dn,S,D,DF);
  k_res<<<S*D/256,256,0,stream>>>(x1,dn,mod,5*D,(u16*)d_out);
}
```

```cpp
#include <hip/hip_runtime.h>
#include <hip/hip_bf16.h>
#include <stdint.h>

typedef unsigned short u16;
typedef __attribute__((ext_vector_type(8))) short bf16x8;
typedef __attribute__((ext_vector_type(4))) short s16x4;
typedef __attribute__((ext_vector_type(4))) float f32x4;
typedef __attribute__((ext_vector_type(2))) unsigned u32x2;
typedef __attribute__((ext_vector_type(4))) unsigned u32x4;
#define LDS_AS __attribute__((address_space(3)))
#define GLB_AS __attribute__((address_space(1)))

__device__ __forceinline__ float bf2f(u16 v){ return __uint_as_float(((unsigned)v)<<16); }
__device__ __forceinline__ u16 f2bf(float f){ unsigned u=__float_as_uint(f); u += 0x7FFF + ((u>>16)&1); return (u16)(u>>16); }
__device__ __forceinline__ float rbf(float f){ return bf2f(f2bf(f)); }
__device__ __forceinline__ float lo_f(unsigned u){ return __uint_as_float(u<<16); }
__device__ __forceinline__ float hi_f(unsigned u){ return __uint_as_float(u & 0xffff0000u); }
__device__ __forceinline__ unsigned pack_bf(float lo, float hi){ return (unsigned)f2bf(lo) | ((unsigned)f2bf(hi)<<16); }

__device__ __forceinline__ void glds16(const void* g, char LDS_AS* l) {
  __builtin_amdgcn_global_load_lds((const GLB_AS void*)g, (LDS_AS void*)l, 16, 0, 0);
}
template<int N> __device__ __forceinline__ void wait_vm(){ asm volatile("s_waitcnt vmcnt(%0)"::"n"(N):"memory"); }
__device__ __forceinline__ void bar(){ __builtin_amdgcn_s_barrier(); }
__device__ __forceinline__ bf16x8 lds_b128(const char LDS_AS* p){ return *(const bf16x8 LDS_AS*)p; }
__device__ __forceinline__ s16x4 lds_tr(const char LDS_AS* p){ return __builtin_amdgcn_ds_read_tr16_b64_v4i16((s16x4 LDS_AS*)p); }
__device__ __forceinline__ bf16x8 cat8(s16x4 a, s16x4 b){ bf16x8 r; r[0]=a[0];r[1]=a[1];r[2]=a[2];r[3]=a[3];r[4]=b[0];r[5]=b[1];r[6]=b[2];r[7]=b[3]; return r; }
#define MFMA16(a,b,c) __builtin_amdgcn_mfma_f32_16x16x32_bf16((a),(b),(c),0,0,0)

constexpr int S=2048, D=512, DF=1792, H=32, DH=16;
template<int K, int NST>
__global__ __launch_bounds__(256) void k_gemm_res(const u16* __restrict__ A, const u16* __restrict__ W,
    const u16* __restrict__ res, const float* __restrict__ gmod, u16* __restrict__ out)
{
  constexpr int NK = K/64;
  constexpr int STB = 16384;
  __shared__ __attribute__((aligned(16))) char smem_[NST*STB];
  char LDS_AS* smem = (char LDS_AS*)smem_;
  const int tid = threadIdx.x, lane = tid & 63, wv = tid >> 6;
  const int b = blockIdx.x;
  const int rb = (b & 7) * 4 + ((b >> 3) & 3);
  const int cb = b >> 5;
  const int m0 = rb * 64, n0 = cb * 64;

  const u16* asrc[2]; const u16* bsrc[2]; int adst[2], bdst[2];
#pragma unroll
  for (int i = 0; i < 2; i++) {
    int inst = wv + 4*i;
    int row = inst*8 + (lane >> 3);
    int slot = lane & 7;
    int ach = slot ^ (row & 7);
    asrc[i] = A + (size_t)(m0 + row) * K + ach * 8;
    int f = ((row >> 1) & 1) | (((row >> 3) & 1) << 1);
    int bch = slot ^ (2*f);
    bsrc[i] = W + (size_t)row * D + n0 + bch * 8;
    adst[i] = inst * 1024;
    bdst[i] = 8192 + inst * 1024;
  }
  auto issue = [&](int kc) {
    char LDS_AS* st = smem + (kc % NST) * STB;
#pragma unroll
    for (int i = 0; i < 2; i++) {
      glds16(asrc[i] + kc * 64, st + adst[i] + lane * 16);
      glds16(bsrc[i] + (size_t)kc * 64 * D, st + bdst[i] + lane * 16);
    }
  };
  const int wm = wv >> 1, wn = wv & 1;
  const int fr = lane & 15, g = lane >> 4, q = (lane & 15) >> 2, p = lane & 3;
  f32x4 acc[2][2] = {};
  int aoff[2];
#pragma unroll
  for (int mt = 0; mt < 2; mt++) { int row = 32*wm + 16*mt + fr; aoff[mt] = row * 128 + ((g ^ (row & 7)) << 4); }
  const int bf = (q >> 1) | ((g & 1) << 1);
  int boff[2];
#pragma unroll
  for (int nt = 0; nt < 2; nt++) { int c = 2*wn + nt; int ch = (2*c + (p >> 1)) ^ (2*bf); boff[nt] = 8192 + (8*g + q) * 128 + ch * 16 + 8 * (p & 1); }

#pragma unroll
  for (int i = 0; i < NST-1; i++) if (i < NK) issue(i);

#pragma unroll 1
  for (int kc = 0; kc < NK - (NST-1); kc++) {
    wait_vm<4*(NST-2)>();
    bar();
    issue(kc + NST - 1);
    const char LDS_AS* st = smem + (kc % NST) * STB;
#pragma unroll
    for (int s = 0; s < 2; s++) {
      bf16x8 af[2], bfr[2];
#pragma unroll
      for (int mt = 0; mt < 2; mt++) af[mt] = lds_b128(st + (aoff[mt] ^ (s << 6)));
#pragma unroll
      for (int nt = 0; nt < 2; nt++) bfr[nt] = cat8(lds_tr(st + boff[nt] + s*32*128), lds_tr(st + boff[nt] + s*32*128 + 4*128));
#pragma unroll
      for (int mt = 0; mt < 2; mt++)
#pragma unroll
        for (int nt = 0; nt < 2; nt++) acc[mt][nt] = MFMA16(bfr[nt], af[mt], acc[mt][nt]);
    }
  }
#pragma unroll
  for (int j = NST-2; j >= 0; j--) {
    int kc = NK - 1 - j;
    if (kc >= 0) {
    switch (j) {
      case 0: wait_vm<0>(); break; case 1: wait_vm<4>(); break; case 2: wait_vm<8>(); break; case 3: wait_vm<12>(); break;
      case 4: wait_vm<16>(); break; case 5: wait_vm<20>(); break; case 6: wait_vm<24>(); break; default: wait_vm<28>(); break;
    }
    bar();
    const char LDS_AS* st = smem + (kc % NST) * STB;
#pragma unroll
    for (int s = 0; s < 2; s++) {
      bf16x8 af[2], bfr[2];
#pragma unroll
      for (int mt = 0; mt < 2; mt++) af[mt] = lds_b128(st + (aoff[mt] ^ (s << 6)));
#pragma unroll
      for (int nt = 0; nt < 2; nt++) bfr[nt] = cat8(lds_tr(st + boff[nt] + s*32*128), lds_tr(st + boff[nt] + s*32*128 + 4*128));
#pragma unroll
      for (int mt = 0; mt < 2; mt++)
#pragma unroll
        for (int nt = 0; nt < 2; nt++) acc[mt][nt] = MFMA16(bfr[nt], af[mt], acc[mt][nt]);
    }
    }
  }
#pragma unroll
  for (int mt = 0; mt < 2; mt++)
#pragma unroll
    for (int nt = 0; nt < 2; nt++) {
      int m = m0 + 32*wm + 16*mt + fr;
      int n = n0 + 32*wn + 16*nt + 4*g;
      u32x2 rv = *(const u32x2*)(res + (size_t)m * D + n);
      f32x4 gv = *(const f32x4*)(gmod + n);
      float o0 = lo_f(rv[0]) + rbf(gv[0] * rbf(acc[mt][nt][0]));
      float o1 = hi_f(rv[0]) + rbf(gv[1] * rbf(acc[mt][nt][1]));
      float o2 = lo_f(rv[1]) + rbf(gv[2] * rbf(acc[mt][nt][2]));
      float o3 = hi_f(rv[1]) + rbf(gv[3] * rbf(acc[mt][nt][3]));
      u32x2 ov; ov[0] = pack_bf(o0, o1); ov[1] = pack_bf(o2, o3);
      *(u32x2*)(out + (size_t)m * D + n) = ov;
    }
}
__global__ void k_mod(const u16* c, const u16* wmod, float* mod){
  int n = blockIdx.x*blockDim.x+threadIdx.x;
  if(n>=6*D) return;
  float acc=0.f;
  for(int k=0;k<D;k++){ float cv=bf2f(c[k]); float s=rbf(cv/(1.f+__expf(-cv))); acc+= s*bf2f(wmod[(size_t)k*6*D+n]); }
  mod[n]=rbf(acc);
}
__global__ void k_ln(const u16* x, const float* mod, int shift_off, int scale_off, u16* h){
  int row=blockIdx.x; int t=threadIdx.x;
  __shared__ float red[256];
  float a=bf2f(x[row*D+t]), b=bf2f(x[row*D+t+256]);
  red[t]=a+b; __syncthreads();
  for(int s=128;s>0;s>>=1){ if(t<s) red[t]+=red[t+s]; __syncthreads(); }
  float mean=red[0]/D; __syncthreads();
  float da=a-mean, db=b-mean;
  red[t]=da*da+db*db; __syncthreads();
  for(int s=128;s>0;s>>=1){ if(t<s) red[t]+=red[t+s]; __syncthreads(); }
  float var=red[0]/D;
  float m=rbf(mean), v=rbf(var);
  float den=rbf(sqrtf(rbf(v+1e-5f)));
  for(int i=0;i<2;i++){
    int d=t+i*256; float xv=i?b:a;
    float n=rbf(rbf(xv-m)/den);
    float sc=rbf(1.f+mod[scale_off+d]);
    float hv=rbf(rbf(n*sc)+mod[shift_off+d]);
    h[row*D+d]=f2bf(hv);
  }
}
__global__ void k_gemm(const u16* A, const u16* W, u16* C, int M, int N, int K){
  __shared__ float sA[16][17], sB[16][17];
  int tx=threadIdx.x, ty=threadIdx.y;
  int row=blockIdx.y*16+ty, col=blockIdx.x*16+tx;
  float acc=0.f;
  for(int k0=0;k0<K;k0+=16){
    sA[ty][tx]=bf2f(A[(size_t)row*K+k0+tx]);
    sB[ty][tx]=bf2f(W[(size_t)(k0+ty)*N+col]);
    __syncthreads();
    for(int k=0;k<16;k++) acc+=sA[ty][k]*sB[k][tx];
    __syncthreads();
  }
  C[(size_t)row*N+col]=f2bf(acc);
}
__global__ void k_attn(const u16* qkv, u16* o){
  int s=blockIdx.x; int h=threadIdx.x;
  if(h>=H) return;
  float q[DH];
  for(int d=0;d<DH;d++) q[d]=bf2f(qkv[(size_t)s*3*D + h*DH+d]);
  float lg[8]; float mx=-1e30f;
  for(int w=0;w<8;w++){
    int idx=s-2*w;
    float l;
    if(idx>=0){ float a=0; for(int d=0;d<DH;d++) a+=q[d]*bf2f(qkv[(size_t)idx*3*D + D + h*DH+d]); l=rbf(rbf(a)*0.25f);} else l=-1e9f;
    lg[w]=l; mx=fmaxf(mx,l);
  }
  float sum=0; for(int w=0;w<8;w++){ lg[w]=__expf(lg[w]-mx); sum+=lg[w]; }
  float ov[DH]; for(int d=0;d<DH;d++) ov[d]=0;
  for(int w=0;w<8;w++){
    int idx=s-2*w; if(idx<0) idx=0;
    float p=rbf(lg[w]/sum);
    for(int d=0;d<DH;d++) ov[d]+=p*bf2f(qkv[(size_t)idx*3*D + 2*D + h*DH+d]);
  }
  for(int d=0;d<DH;d++) o[(size_t)s*D+h*DH+d]=f2bf(ov[d]);
}
__global__ void k_res(const u16* x, const u16* y, const float* mod, int goff, u16* out){
  int i=blockIdx.x*blockDim.x+threadIdx.x; if(i>=S*D) return;
  int d=i%D;
  out[i]=f2bf(bf2f(x[i]) + rbf(mod[goff+d]*bf2f(y[i])));
}
__global__ void k_glu(const u16* g, const u16* u, u16* hh){
  int i=blockIdx.x*blockDim.x+threadIdx.x; if(i>=S*DF) return;
  float gv=bf2f(g[i]);
  float t=tanhf(0.7978845608028654f*(gv+0.044715f*gv*gv*gv));
  float ge=rbf(0.5f*gv*(1.f+t));
  hh[i]=f2bf(ge*bf2f(u[i]));
}

extern "C" void kernel_launch(void* const* d_in, const int* in_sizes, int n_in,
                              void* d_out, int out_size, void* d_ws, size_t ws_size,
                              hipStream_t stream) {
  const u16* x=(const u16*)d_in[0]; const u16* wqkv=(const u16*)d_in[1]; const u16* wo=(const u16*)d_in[2];
  const u16* wg=(const u16*)d_in[4]; const u16* wu=(const u16*)d_in[5]; const u16* wd=(const u16*)d_in[6];
  const u16* c=(const u16*)d_in[7]; const u16* wmod=(const u16*)d_in[8];
  char* ws=(char*)d_ws;
  float* mod=(float*)ws; ws+=16384;
  u16* h=(u16*)ws; ws+=S*D*2;
  u16* qkv=(u16*)ws; ws+=S*3*D*2;
  u16* o=(u16*)ws; ws+=S*D*2;
  u16* ao=(u16*)ws; ws+=S*D*2;
  u16* x1=(u16*)ws; ws+=S*D*2;
  u16* g=(u16*)ws; ws+=S*DF*2;
  u16* u=(u16*)ws; ws+=S*DF*2;
  u16* hh=(u16*)ws; ws+=S*DF*2;
  u16* dn=(u16*)ws; ws+=S*D*2;
  k_mod<<<12,256,0,stream>>>(c,wmod,mod);
  k_ln<<<S,256,0,stream>>>(x,mod,0,D,h);
  k_gemm<<<dim3(3*D/16,S/16),dim3(16,16),0,stream>>>(h,wqkv,qkv,S,3*D,D);
  k_attn<<<S,64,0,stream>>>(qkv,o);
  k_gemm_res<512,6><<<256,256,0,stream>>>(o,wo,x,mod+2*D,x1);
  k_ln<<<S,256,0,stream>>>(x1,mod,3*D,4*D,h);
  k_gemm<<<dim3(DF/16,S/16),dim3(16,16),0,stream>>>(h,wg,g,S,DF,D);
  k_gemm<<<dim3(DF/16,S/16),dim3(16,16),0,stream>>>(h,wu,u,S,DF,D);
  k_glu<<<S*DF/256,256,0,stream>>>(g,u,hh);
  k_gemm_res<1792,6><<<256,256,0,stream>>>(hh,wd,x1,mod+5*D,(u16*)d_out);
}
```

```cpp
#include <hip/hip_runtime.h>
#include <hip/hip_bf16.h>
#include <stdint.h>

typedef unsigned short u16;
typedef __attribute__((ext_vector_type(8))) short bf16x8;
typedef __attribute__((ext_vector_type(4))) short s16x4;
typedef __attribute__((ext_vector_type(4))) float f32x4;
typedef __attribute__((ext_vector_type(2))) unsigned u32x2;
typedef __attribute__((ext_vector_type(4))) unsigned u32x4;
#define LDS_AS __attribute__((address_space(3)))
#define GLB_AS __attribute__((address_space(1)))

__device__ __forceinline__ float bf2f(u16 v){ return __uint_as_float(((unsigned)v)<<16); }
__device__ __forceinline__ u16 f2bf(float f){ unsigned u=__float_as_uint(f); u += 0x7FFF + ((u>>16)&1); return (u16)(u>>16); }
__device__ __forceinline__ float rbf(float f){ return bf2f(f2bf(f)); }
__device__ __forceinline__ float lo_f(unsigned u){ return __uint_as_float(u<<16); }
__device__ __forceinline__ float hi_f(unsigned u){ return __uint_as_float(u & 0xffff0000u); }
__device__ __forceinline__ unsigned pack_bf(float lo, float hi){ return (unsigned)f2bf(lo) | ((unsigned)f2bf(hi)<<16); }

__device__ __forceinline__ void glds16(const void* g, char LDS_AS* l) {
  __builtin_amdgcn_global_load_lds((const GLB_AS void*)g, (LDS_AS void*)l, 16, 0, 0);
}
template<int N> __device__ __forceinline__ void wait_vm(){ asm volatile("s_waitcnt vmcnt(%0)"::"n"(N):"memory"); }
__device__ __forceinline__ void bar(){ __builtin_amdgcn_s_barrier(); }
__device__ __forceinline__ bf16x8 lds_b128(const char LDS_AS* p){ return *(const bf16x8 LDS_AS*)p; }
__device__ __forceinline__ s16x4 lds_tr(const char LDS_AS* p){ return __builtin_amdgcn_ds_read_tr16_b64_v4i16((s16x4 LDS_AS*)p); }
__device__ __forceinline__ bf16x8 cat8(s16x4 a, s16x4 b){ bf16x8 r; r[0]=a[0];r[1]=a[1];r[2]=a[2];r[3]=a[3];r[4]=b[0];r[5]=b[1];r[6]=b[2];r[7]=b[3]; return r; }
#define MFMA16(a,b,c) __builtin_amdgcn_mfma_f32_16x16x32_bf16((a),(b),(c),0,0,0)

constexpr int S=2048, D=512, DF=1792, H=32, DH=16;
template<int K, int NST>
__global__ __launch_bounds__(256) void k_gemm_res(const u16* __restrict__ A, const u16* __restrict__ W,
    const u16* __restrict__ res, const float* __restrict__ gmod, u16* __restrict__ out)
{
  constexpr int NK = K/64;
  constexpr int STB = 16384;
  __shared__ __attribute__((aligned(16))) char smem_[NST*STB];
  char LDS_AS* smem = (char LDS_AS*)smem_;
  const int tid = threadIdx.x, lane = tid & 63, wv = tid >> 6;
  const int b = blockIdx.x;
  const int rb = (b & 7) * 4 + ((b >> 3) & 3);
  const int cb = b >> 5;
  const int m0 = rb * 64, n0 = cb * 64;

  const u16* asrc[2]; const u16* bsrc[2]; int adst[2], bdst[2];
#pragma unroll
  for (int i = 0; i < 2; i++) {
    int inst = wv + 4*i;
    int row = inst*8 + (lane >> 3);
    int slot = lane & 7;
    int ach = slot ^ (row & 7);
    asrc[i] = A + (size_t)(m0 + row) * K + ach * 8;
    int f = ((row >> 1) & 1) | (((row >> 3) & 1) << 1);
    int bch = slot ^ (2*f);
    bsrc[i] = W + (size_t)row * D + n0 + bch * 8;
    adst[i] = inst * 1024;
    bdst[i] = 8192 + inst * 1024;
  }
  auto issue = [&](int kc) {
    char LDS_AS* st = smem + (kc % NST) * STB;
#pragma unroll
    for (int i = 0; i < 2; i++) {
      glds16(asrc[i] + kc * 64, st + adst[i] + lane * 16);
      glds16(bsrc[i] + (size_t)kc * 64 * D, st + bdst[i] + lane * 16);
    }
  };
  const int wm = wv >> 1, wn = wv & 1;
  const int fr = lane & 15, g = lane >> 4, q = (lane & 15) >> 2, p = lane & 3;
  f32x4 acc[2][2] = {};
  int aoff[2];
#pragma unroll
  for (int mt = 0; mt < 2; mt++) { int row = 32*wm + 16*mt + fr; aoff[mt] = row * 128 + ((g ^ (row & 7)) << 4); }
  const int bf = (q >> 1) | ((g & 1) << 1);
  int boff[2];
#pragma unroll
  for (int nt = 0; nt < 2; nt++) { int c = 2*wn + nt; int ch = (2*c + (p >> 1)) ^ (2*bf); boff[nt] = 8192 + (8*g + q) * 128 + ch * 16 + 8 * (p & 1); }

#pragma unroll
  for (int i = 0; i < NST-1; i++) if (i < NK) issue(i);

#pragma unroll 1
  for (int kc = 0; kc < NK - (NST-1); kc++) {
    wait_vm<4*(NST-2)>();
    bar();
    issue(kc + NST - 1);
    const char LDS_AS* st = smem + (kc % NST) * STB;
#pragma unroll
    for (int s = 0; s < 2; s++) {
      bf16x8 af[2], bfr[2];
#pragma unroll
      for (int mt = 0; mt < 2; mt++) af[mt] = lds_b128(st + (aoff[mt] ^ (s << 6)));
#pragma unroll
      for (int nt = 0; nt < 2; nt++) bfr[nt] = cat8(lds_tr(st + boff[nt] + s*32*128), lds_tr(st + boff[nt] + s*32*128 + 4*128));
#pragma unroll
      for (int mt = 0; mt < 2; mt++)
#pragma unroll
        for (int nt = 0; nt < 2; nt++) acc[mt][nt] = MFMA16(bfr[nt], af[mt], acc[mt][nt]);
    }
  }
#pragma unroll
  for (int j = NST-2; j >= 0; j--) {
    int kc = NK - 1 - j;
    if (kc >= 0) {
    switch (j) {
      case 0: wait_vm<0>(); break; case 1: wait_vm<4>(); break; case 2: wait_vm<8>(); break; case 3: wait_vm<12>(); break;
      case 4: wait_vm<16>(); break; case 5: wait_vm<20>(); break; case 6: wait_vm<24>(); break; default: wait_vm<28>(); break;
    }
    bar();
    const char LDS_AS* st = smem + (kc % NST) * STB;
#pragma unroll
    for (int s = 0; s < 2; s++) {
      bf16x8 af[2], bfr[2];
#pragma unroll
      for (int mt = 0; mt < 2; mt++) af[mt] = lds_b128(st + (aoff[mt] ^ (s << 6)));
#pragma unroll
      for (int nt = 0; nt < 2; nt++) bfr[nt] = cat8(lds_tr(st + boff[nt] + s*32*128), lds_tr(st + boff[nt] + s*32*128 + 4*128));
#pragma unroll
      for (int mt = 0; mt < 2; mt++)
#pragma unroll
        for (int nt = 0; nt < 2; nt++) acc[mt][nt] = MFMA16(bfr[nt], af[mt], acc[mt][nt]);
    }
    }
  }
#pragma unroll
  for (int mt = 0; mt < 2; mt++)
#pragma unroll
    for (int nt = 0; nt < 2; nt++) {
      int m = m0 + 32*wm + 16*mt + fr;
      int n = n0 + 32*wn + 16*nt + 4*g;
      u32x2 rv = *(const u32x2*)(res + (size_t)m * D + n);
      f32x4 gv = *(const f32x4*)(gmod + n);
      float o0 = lo_f(rv[0]) + rbf(gv[0] * rbf(acc[mt][nt][0]));
      float o1 = hi_f(rv[0]) + rbf(gv[1] * rbf(acc[mt][nt][1]));
      float o2 = lo_f(rv[1]) + rbf(gv[2] * rbf(acc[mt][nt][2]));
      float o3 = hi_f(rv[1]) + rbf(gv[3] * rbf(acc[mt][nt][3]));
      u32x2 ov; ov[0] = pack_bf(o0, o1); ov[1] = pack_bf(o2, o3);
      *(u32x2*)(out + (size_t)m * D + n) = ov;
    }
}
typedef __attribute__((ext_vector_type(2))) __bf16 bf16x2_t;
__device__ __forceinline__ float dot2bf(unsigned a, unsigned b, float c){
  return __builtin_amdgcn_fdot2_f32_bf16(__builtin_bit_cast(bf16x2_t,a), __builtin_bit_cast(bf16x2_t,b), c, false);
}
__device__ __forceinline__ void ln_stats(const u32x4 (&v)[8], float& mean_b, float& rden) {
  float s = 0.f;
#pragma unroll
  for (int j = 0; j < 8; j++)
#pragma unroll
    for (int e = 0; e < 4; e++) s += lo_f(v[j][e]) + hi_f(v[j][e]);
  s += __shfl_xor(s, 1); s += __shfl_xor(s, 2); s += __shfl_xor(s, 4);
  float mean = s * (1.f/D);
  float q = 0.f;
#pragma unroll
  for (int j = 0; j < 8; j++)
#pragma unroll
    for (int e = 0; e < 4; e++) { float a = lo_f(v[j][e]) - mean, b = hi_f(v[j][e]) - mean; q += a*a + b*b; }
  q += __shfl_xor(q, 1); q += __shfl_xor(q, 2); q += __shfl_xor(q, 4);
  float var = rbf(q * (1.f/D));
  mean_b = rbf(mean);
  rden = 1.f / rbf(sqrtf(rbf(var + 1e-5f)));
}
__device__ __forceinline__ unsigned ln_mod2(unsigned xv, float m, float rden, float sc0, float sh0, float sc1, float sh1) {
  float n0 = rbf(rbf(lo_f(xv) - m) * rden), n1 = rbf(rbf(hi_f(xv) - m) * rden);
  return pack_bf(rbf(n0 * sc0) + sh0, rbf(n1 * sc1) + sh1);
}

__global__ __launch_bounds__(256) void k_qkv_attn(const u16* __restrict__ x, const float* __restrict__ mod,
    const u16* __restrict__ W, u16* __restrict__ o)
{
  constexpr int AB = 80*1024, STB = 24576, NST = 3, NK = 8;
  __shared__ __attribute__((aligned(16))) char smem_[AB + NST*STB];
  char LDS_AS* smem = (char LDS_AS*)smem_;
  const int tid = threadIdx.x, lane = tid & 63, wv = tid >> 6;
  const int hg = blockIdx.x & 7, rb = blockIdx.x >> 3;
  const int r0 = rb * 64;

  const u16* bsrc[6];
#pragma unroll
  for (int i = 0; i < 6; i++) {
    int L = (wv + 4*i) * 64 + lane;
    int row = L / 24, cc = L % 24, seg = cc >> 3;
    int f = ((row >> 1) & 1) | (((row >> 3) & 1) << 1);
    int ch = (cc & 7) ^ (2*f);
    bsrc[i] = W + (size_t)row * 1536 + seg * 512 + hg * 64 + ch * 8;
  }
  auto issue = [&](int kc) {
    char LDS_AS* st = smem + AB + (kc % NST) * STB;
#pragma unroll
    for (int i = 0; i < 6; i++) glds16(bsrc[i] + (size_t)kc * 64 * 1536, st + (wv + 4*i) * 1024 + lane * 16);
  };
  issue(0); issue(1);

  {
    const int sub = tid & 7, rl = tid >> 3;
    u32x4 v[3][8];
#pragma unroll
    for (int ps = 0; ps < 3; ps++) {
      int gr = r0 - 16 + ps*32 + rl; gr = gr < 0 ? 0 : (gr > S-1 ? S-1 : gr);
      const u32x4* src = (const u32x4*)(x + (size_t)gr * D);
#pragma unroll
      for (int j = 0; j < 8; j++) v[ps][j] = src[sub + 8*j];
    }
    float mean[3], rden[3];
#pragma unroll
    for (int ps = 0; ps < 3; ps++) ln_stats(v[ps], mean[ps], rden[ps]);
#pragma unroll
    for (int j = 0; j < 8; j++) {
      int col = (sub + 8*j) * 8;
      f32x4 sa0 = *(const f32x4*)(mod + col), sa1 = *(const f32x4*)(mod + col + 4);
      f32x4 ca0 = *(const f32x4*)(mod + D + col), ca1 = *(const f32x4*)(mod + D + col + 4);
      float sc[8], sh[8];
#pragma unroll
      for (int e = 0; e < 4; e++) { sc[e] = rbf(1.f + ca0[e]); sc[4+e] = rbf(1.f + ca1[e]); sh[e] = sa0[e]; sh[4+e] = sa1[e]; }
#pragma unroll
      for (int ps = 0; ps < 3; ps++) {
        int row = ps*32 + rl;
        u32x4 h;
#pragma unroll
        for (int e = 0; e < 4; e++) h[e] = ln_mod2(v[ps][j][e], mean[ps], rden[ps], sc[2*e], sh[2*e], sc[2*e+1], sh[2*e+1]);
        if (row < 80) *(u32x4 LDS_AS*)(smem + row * 1024 + (((sub + 8*j) ^ ((row & 7) << 1)) << 4)) = h;
      }
    }
  }
  const int fr = lane & 15, g = lane >> 4, q = (lane & 15) >> 2, p = lane & 3;
  f32x4 acc[5][3] = {};
  int aoff[5];
#pragma unroll
  for (int mt = 0; mt < 5; mt++) { int row = 16*mt + fr; aoff[mt] = row * 1024 + ((g ^ ((row & 7) << 1)) << 4); }
  const int bf = (q >> 1) | ((g & 1) << 1);
  int boff[3];
#pragma unroll
  for (int nt = 0; nt < 3; nt++) { int c = 3*wv + nt; int cl = 2*c + (p >> 1); int ph = (cl & ~7) | ((cl & 7) ^ (2*bf)); boff[nt] = (8*g + q) * 384 + ph * 16 + 8 * (p & 1); }

#pragma unroll
  for (int kc = 0; kc < NK; kc++) {
    if (kc < NK-1) wait_vm<6>(); else wait_vm<0>();
    if (kc == 0) __syncthreads(); else bar();
    if (kc + 2 < NK) issue(kc + 2);
    const char LDS_AS* st = smem + AB + (kc % NST) * STB;
#pragma unroll
    for (int s = 0; s < 2; s++) {
      bf16x8 bfr[3];
#pragma unroll
      for (int nt = 0; nt < 3; nt++) bfr[nt] = cat8(lds_tr(st + boff[nt] + s*32*384), lds_tr(st + boff[nt] + s*32*384 + 4*384));
#pragma unroll
      for (int mt = 0; mt < 5; mt++) {
        bf16x8 af = lds_b128(smem + (aoff[mt] ^ ((2*kc + s) << 6)));
#pragma unroll
        for (int nt = 0; nt < 3; nt++) acc[mt][nt] = MFMA16(bfr[nt], af, acc[mt][nt]);
      }
    }
  }
  __syncthreads();
  constexpr int RS = 400;
#pragma unroll
  for (int mt = 0; mt < 5; mt++)
#pragma unroll
    for (int nt = 0; nt < 3; nt++) {
      int row = 16*mt + fr, col = 48*wv + 16*nt + 4*g;
      u32x2 w2; w2[0] = pack_bf(acc[mt][nt][0], acc[mt][nt][1]); w2[1] = pack_bf(acc[mt][nt][2], acc[mt][nt][3]);
      *(u32x2 LDS_AS*)(smem + row * RS + col * 2) = w2;
    }
  __syncthreads();
  {
    const int row = tid >> 2, hd = tid & 3;
    const int srow = r0 + row;
    const char LDS_AS* qp = smem + (16 + row) * RS + hd * 32;
    u32x4 q0 = *(const u32x4 LDS_AS*)qp, q1 = *(const u32x4 LDS_AS*)(qp + 16);
    float lg[8]; float mx = -3.0e38f;
#pragma unroll
    for (int w = 0; w < 8; w++) {
      const char LDS_AS* kp = smem + (16 + row - 2*w) * RS + 128 + hd * 32;
      u32x4 k0 = *(const u32x4 LDS_AS*)kp, k1 = *(const u32x4 LDS_AS*)(kp + 16);
      float a = 0.f;
#pragma unroll
      for (int e = 0; e < 4; e++) a = dot2bf(q0[e], k0[e], a);
#pragma unroll
      for (int e = 0; e < 4; e++) a = dot2bf(q1[e], k1[e], a);
      float l = rbf(a) * 0.25f;
      l = (srow - 2*w >= 0) ? l : -1.0e9f;
      lg[w] = l; mx = fmaxf(mx, l);
    }
    float sum = 0.f;
#pragma unroll
    for (int w = 0; w < 8; w++) { lg[w] = __expf(lg[w] - mx); sum += lg[w]; }
    float rs = 1.f / sum;
    float ov[16];
#pragma unroll
    for (int d = 0; d < 16; d++) ov[d] = 0.f;
#pragma unroll
    for (int w = 0; w < 8; w++) {
      const char LDS_AS* vp = smem + (16 + row - 2*w) * RS + 256 + hd * 32;
      u32x4 v0 = *(const u32x4 LDS_AS*)vp, v1 = *(const u32x4 LDS_AS*)(vp + 16);
      float pw = rbf(lg[w] * rs);
#pragma unroll
      for (int e = 0; e < 4; e++) {
        ov[2*e]   += pw * lo_f(v0[e]); ov[2*e+1]   += pw * hi_f(v0[e]);
        ov[8+2*e] += pw * lo_f(v1[e]); ov[8+2*e+1] += pw * hi_f(v1[e]);
      }
    }
    u32x4 o0, o1;
#pragma unroll
    for (int e = 0; e < 4; e++) { o0[e] = pack_bf(ov[2*e], ov[2*e+1]); o1[e] = pack_bf(ov[8+2*e], ov[8+2*e+1]); }
    u32x4* dst = (u32x4*)(o + (size_t)srow * D + hg * 64 + hd * 16);
    dst[0] = o0; dst[1] = o1;
  }
}
__global__ __launch_bounds__(512) void k_ln_glu(const u16* __restrict__ x1, const float* __restrict__ mod,
    const u16* __restrict__ Wg, const u16* __restrict__ Wu, u16* __restrict__ hh)
{
  constexpr int AB = 128*1024, STB = 16384, NST = 2, NK = 16;
  __shared__ __attribute__((aligned(16))) char smem_[AB + NST*STB];
  char LDS_AS* smem = (char LDS_AS*)smem_;
  const int tid = threadIdx.x, lane = tid & 63, wv = tid >> 6;
  const int b = blockIdx.x;
  const int rb = b & 15, cb = b >> 4;
  const int r0 = rb * 128, c0 = cb * 128;

  const u16* bsrc[2];
#pragma unroll
  for (int i = 0; i < 2; i++) {
    int L = (wv + 8*i) * 64 + lane;
    int row = L >> 5, cc = L & 31;
    int f = (row & 3) | (((row >> 3) & 1) << 2);
    int ch = cc ^ (2*f);
    const u16* base = (ch < 16) ? Wg : Wu;
    bsrc[i] = base + (size_t)row * DF + c0 + (ch & 15) * 8;
  }
  auto issue = [&](int kc) {
    char LDS_AS* st = smem + AB + (kc % NST) * STB;
#pragma unroll
    for (int i = 0; i < 2; i++) glds16(bsrc[i] + (size_t)kc * 32 * DF, st + (wv + 8*i) * 1024 + lane * 16);
  };
  issue(0);
  {
    const int sub = tid & 7, rl = tid >> 3;
    u32x4 v[2][8];
#pragma unroll
    for (int ps = 0; ps < 2; ps++) {
      const u32x4* src = (const u32x4*)(x1 + (size_t)(r0 + ps*64 + rl) * D);
#pragma unroll
      for (int j = 0; j < 8; j++) v[ps][j] = src[sub + 8*j];
    }
    float mean[2], rden[2];
#pragma unroll
    for (int ps = 0; ps < 2; ps++) ln_stats(v[ps], mean[ps], rden[ps]);
#pragma unroll
    for (int j = 0; j < 8; j++) {
      int col = (sub + 8*j) * 8;
      f32x4 sa0 = *(const f32x4*)(mod + 3*D + col), sa1 = *(const f32x4*)(mod + 3*D + col + 4);
      f32x4 ca0 = *(const f32x4*)(mod + 4*D + col), ca1 = *(const f32x4*)(mod + 4*D + col + 4);
      float sc[8], sh[8];
#pragma unroll
      for (int e = 0; e < 4; e++) { sc[e] = rbf(1.f + ca0[e]); sc[4+e] = rbf(1.f + ca1[e]); sh[e] = sa0[e]; sh[4+e] = sa1[e]; }
#pragma unroll
      for (int ps = 0; ps < 2; ps++) {
        int row = ps*64 + rl;
        u32x4 h;
#pragma unroll
        for (int e = 0; e < 4; e++) h[e] = ln_mod2(v[ps][j][e], mean[ps], rden[ps], sc[2*e], sh[2*e], sc[2*e+1], sh[2*e+1]);
        *(u32x4 LDS_AS*)(smem + row * 1024 + (((sub + 8*j) ^ ((row & 7) << 1)) << 4)) = h;
      }
    }
  }
  const int wm = wv >> 2, wn = wv & 3;
  const int fr = lane & 15, g = lane >> 4, q = (lane & 15) >> 2, p = lane & 3;
  f32x4 acc[4][4] = {};
  int aoff[4];
#pragma unroll
  for (int mt = 0; mt < 4; mt++) { int row = 64*wm + 16*mt + fr; aoff[mt] = row * 1024 + ((g ^ ((row & 7) << 1)) << 4); }
  const int bf = q | ((g & 1) << 2);
  int boff[4];
#pragma unroll
  for (int nt = 0; nt < 4; nt++) { int c = (nt >> 1) * 8 + 2*wn + (nt & 1); int cl = 2*c + (p >> 1); int ph = cl ^ (2*bf); boff[nt] = (8*g + q) * 512 + ph * 16 + 8 * (p & 1); }

#pragma unroll
  for (int kc = 0; kc < NK; kc++) {
    wait_vm<0>();
    if (kc == 0) __syncthreads(); else bar();
    if (kc + 1 < NK) issue(kc + 1);
    const char LDS_AS* st = smem + AB + (kc % NST) * STB;
    bf16x8 bfr[4];
#pragma unroll
    for (int nt = 0; nt < 4; nt++) bfr[nt] = cat8(lds_tr(st + boff[nt]), lds_tr(st + boff[nt] + 4*512));
#pragma unroll
    for (int mt = 0; mt < 4; mt++) {
      bf16x8 af = lds_b128(smem + (aoff[mt] ^ (kc << 6)));
#pragma unroll
      for (int nt = 0; nt < 4; nt++) acc[mt][nt] = MFMA16(bfr[nt], af, acc[mt][nt]);
    }
  }
#pragma unroll
  for (int mt = 0; mt < 4; mt++)
#pragma unroll
    for (int nt = 0; nt < 2; nt++) {
      int m = r0 + 64*wm + 16*mt + fr;
      int n = c0 + 32*wn + 16*nt + 4*g;
      float hv[4];
#pragma unroll
      for (int e = 0; e < 4; e++) {
        float gv = rbf(acc[mt][nt][e]), uv = rbf(acc[mt][2+nt][e]);
        float z = 0.7978845608028654f * (gv + 0.044715f * gv * gv * gv);
        float t = 1.f - 2.f / (1.f + __expf(2.f * z));
        hv[e] = rbf(0.5f * gv * (1.f + t)) * uv;
      }
      u32x2 ov; ov[0] = pack_bf(hv[0], hv[1]); ov[1] = pack_bf(hv[2], hv[3]);
      *(u32x2*)(hh + (size_t)m * DF + n) = ov;
    }
}
__global__ void k_mod(const u16* c, const u16* wmod, float* mod){
  int n = blockIdx.x*blockDim.x+threadIdx.x;
  if(n>=6*D) return;
  float acc=0.f;
  for(int k=0;k<D;k++){ float cv=bf2f(c[k]); float s=rbf(cv/(1.f+__expf(-cv))); acc+= s*bf2f(wmod[(size_t)k*6*D+n]); }
  mod[n]=rbf(acc);
}
__global__ void k_ln(const u16* x, const float* mod, int shift_off, int scale_off, u16* h){
  int row=blockIdx.x; int t=threadIdx.x;
  __shared__ float red[256];
  float a=bf2f(x[row*D+t]), b=bf2f(x[row*D+t+256]);
  red[t]=a+b; __syncthreads();
  for(int s=128;s>0;s>>=1){ if(t<s) red[t]+=red[t+s]; __syncthreads(); }
  float mean=red[0]/D; __syncthreads();
  float da=a-mean, db=b-mean;
  red[t]=da*da+db*db; __syncthreads();
  for(int s=128;s>0;s>>=1){ if(t<s) red[t]+=red[t+s]; __syncthreads(); }
  float var=red[0]/D;
  float m=rbf(mean), v=rbf(var);
  float den=rbf(sqrtf(rbf(v+1e-5f)));
  for(int i=0;i<2;i++){
    int d=t+i*256; float xv=i?b:a;
    float n=rbf(rbf(xv-m)/den);
    float sc=rbf(1.f+mod[scale_off+d]);
    float hv=rbf(rbf(n*sc)+mod[shift_off+d]);
    h[row*D+d]=f2bf(hv);
  }
}
__global__ void k_gemm(const u16* A, const u16* W, u16* C, int M, int N, int K){
  __shared__ float sA[16][17], sB[16][17];
  int tx=threadIdx.x, ty=threadIdx.y;
  int row=blockIdx.y*16+ty, col=blockIdx.x*16+tx;
  float acc=0.f;
  for(int k0=0;k0<K;k0+=16){
    sA[ty][tx]=bf2f(A[(size_t)row*K+k0+tx]);
    sB[ty][tx]=bf2f(W[(size_t)(k0+ty)*N+col]);
    __syncthreads();
    for(int k=0;k<16;k++) acc+=sA[ty][k]*sB[k][tx];
    __syncthreads();
  }
  C[(size_t)row*N+col]=f2bf(acc);
}
__global__ void k_attn(const u16* qkv, u16* o){
  int s=blockIdx.x; int h=threadIdx.x;
  if(h>=H) return;
  float q[DH];
  for(int d=0;d<DH;d++) q[d]=bf2f(qkv[(size_t)s*3*D + h*DH+d]);
  float lg[8]; float mx=-1e30f;
  for(int w=0;w<8;w++){
    int idx=s-2*w;
    float l;
    if(idx>=0){ float a=0; for(int d=0;d<DH;d++) a+=q[d]*bf2f(qkv[(size_t)idx*3*D + D + h*DH+d]); l=rbf(rbf(a)*0.25f);} else l=-1e9f;
    lg[w]=l; mx=fmaxf(mx,l);
  }
  float sum=0; for(int w=0;w<8;w++){ lg[w]=__expf(lg[w]-mx); sum+=lg[w]; }
  float ov[DH]; for(int d=0;d<DH;d++) ov[d]=0;
  for(int w=0;w<8;w++){
    int idx=s-2*w; if(idx<0) idx=0;
    float p=rbf(lg[w]/sum);
    for(int d=0;d<DH;d++) ov[d]+=p*bf2f(qkv[(size_t)idx*3*D + 2*D + h*DH+d]);
  }
  for(int d=0;d<DH;d++) o[(size_t)s*D+h*DH+d]=f2bf(ov[d]);
}
__global__ void k_res(const u16* x, const u16* y, const float* mod, int goff, u16* out){
  int i=blockIdx.x*blockDim.x+threadIdx.x; if(i>=S*D) return;
  int d=i%D;
  out[i]=f2bf(bf2f(x[i]) + rbf(mod[goff+d]*bf2f(y[i])));
}
__global__ void k_glu(const u16* g, const u16* u, u16* hh){
  int i=blockIdx.x*blockDim.x+threadIdx.x; if(i>=S*DF) return;
  float gv=bf2f(g[i]);
  float t=tanhf(0.7978845608028654f*(gv+0.044715f*gv*gv*gv));
  float ge=rbf(0.5f*gv*(1.f+t));
  hh[i]=f2bf(ge*bf2f(u[i]));
}

extern "C" void kernel_launch(void* const* d_in, const int* in_sizes, int n_in,
                              void* d_out, int out_size, void* d_ws, size_t ws_size,
                              hipStream_t stream) {
  const u16* x=(const u16*)d_in[0]; const u16* wqkv=(const u16*)d_in[1]; const u16* wo=(const u16*)d_in[2];
  const u16* wg=(const u16*)d_in[4]; const u16* wu=(const u16*)d_in[5]; const u16* wd=(const u16*)d_in[6];
  const u16* c=(const u16*)d_in[7]; const u16* wmod=(const u16*)d_in[8];
  char* ws=(char*)d_ws;
  float* mod=(float*)ws; ws+=16384;
  u16* h=(u16*)ws; ws+=S*D*2;
  u16* qkv=(u16*)ws; ws+=S*3*D*2;
  u16* o=(u16*)ws; ws+=S*D*2;
  u16* ao=(u16*)ws; ws+=S*D*2;
  u16* x1=(u16*)ws; ws+=S*D*2;
  u16* g=(u16*)ws; ws+=S*DF*2;
  u16* u=(u16*)ws; ws+=S*DF*2;
  u16* hh=(u16*)ws; ws+=S*DF*2;
  u16* dn=(u16*)ws; ws+=S*D*2;
  k_mod<<<12,256,0,stream>>>(c,wmod,mod);
  k_qkv_attn<<<256,256,0,stream>>>(x,mod,wqkv,o);
  k_gemm_res<512,6><<<256,256,0,stream>>>(o,wo,x,mod+2*D,x1);
  k_ln_glu<<<224,512,0,stream>>>(x1,mod,wg,wu,hh);
  k_gemm_res<1792,6><<<256,256,0,stream>>>(hh,wd,x1,mod+5*D,(u16*)d_out);
}
```

```cpp
#include <hip/hip_runtime.h>
#include <hip/hip_bf16.h>
#include <stdint.h>

typedef unsigned short u16;
typedef __attribute__((ext_vector_type(8))) short bf16x8;
typedef __attribute__((ext_vector_type(4))) short s16x4;
typedef __attribute__((ext_vector_type(4))) float f32x4;
typedef __attribute__((ext_vector_type(2))) unsigned u32x2;
typedef __attribute__((ext_vector_type(4))) unsigned u32x4;
#define LDS_AS __attribute__((address_space(3)))
#define GLB_AS __attribute__((address_space(1)))

__device__ __forceinline__ float bf2f(u16 v){ return __uint_as_float(((unsigned)v)<<16); }
__device__ __forceinline__ u16 f2bf(float f){ unsigned u=__float_as_uint(f); u += 0x7FFF + ((u>>16)&1); return (u16)(u>>16); }
__device__ __forceinline__ float rbf(float f){ return bf2f(f2bf(f)); }
__device__ __forceinline__ float lo_f(unsigned u){ return __uint_as_float(u<<16); }
__device__ __forceinline__ float hi_f(unsigned u){ return __uint_as_float(u & 0xffff0000u); }
__device__ __forceinline__ unsigned pack_bf(float lo, float hi){ return (unsigned)f2bf(lo) | ((unsigned)f2bf(hi)<<16); }

__device__ __forceinline__ void glds16(const void* g, char LDS_AS* l) {
  __builtin_amdgcn_global_load_lds((const GLB_AS void*)g, (LDS_AS void*)l, 16, 0, 0);
}
template<int N> __device__ __forceinline__ void wait_vm(){ asm volatile("s_waitcnt vmcnt(%0)"::"n"(N):"memory"); }
__device__ __forceinline__ void bar(){ __builtin_amdgcn_s_barrier(); }
__device__ __forceinline__ bf16x8 lds_b128(const char LDS_AS* p){ return *(const bf16x8 LDS_AS*)p; }
__device__ __forceinline__ s16x4 lds_tr(const char LDS_AS* p){ return __builtin_amdgcn_ds_read_tr16_b64_v4i16((s16x4 LDS_AS*)p); }
__device__ __forceinline__ bf16x8 cat8(s16x4 a, s16x4 b){ bf16x8 r; r[0]=a[0];r[1]=a[1];r[2]=a[2];r[3]=a[3];r[4]=b[0];r[5]=b[1];r[6]=b[2];r[7]=b[3]; return r; }
#define MFMA16(a,b,c) __builtin_amdgcn_mfma_f32_16x16x32_bf16((a),(b),(c),0,0,0)

constexpr int S=2048, D=512, DF=1792, H=32, DH=16;
template<int K, int NST>
__global__ __launch_bounds__(256) void k_gemm_res(const u16* __restrict__ A, const u16* __restrict__ W,
    const u16* __restrict__ res, const float* __restrict__ gmod, u16* __restrict__ out)
{
  constexpr int NK = K/64;
  constexpr int STB = 16384;
  __shared__ __attribute__((aligned(16))) char smem_[NST*STB];
  char LDS_AS* smem = (char LDS_AS*)smem_;
  const int tid = threadIdx.x, lane = tid & 63, wv = tid >> 6;
  const int b = blockIdx.x;
  const int rb = (b & 7) * 4 + ((b >> 3) & 3);
  const int cb = b >> 5;
  const int m0 = rb * 64, n0 = cb * 64;

  const u16* asrc[2]; const u16* bsrc[2]; int adst[2], bdst[2];
#pragma unroll
  for (int i = 0; i < 2; i++) {
    int inst = wv + 4*i;
    int row = inst*8 + (lane >> 3);
    int slot = lane & 7;
    int ach = slot ^ (row & 7);
    asrc[i] = A + (size_t)(m0 + row) * K + ach * 8;
    int f = ((row >> 1) & 1) | (((row >> 3) & 1) << 1);
    int bch = slot ^ (2*f);
    bsrc[i] = W + (size_t)row * D + n0 + bch * 8;
    adst[i] = inst * 1024;
    bdst[i] = 8192 + inst * 1024;
  }
  auto issue = [&](int kc) {
    char LDS_AS* st = smem + (kc % NST) * STB;
#pragma unroll
    for (int i = 0; i < 2; i++) {
      glds16(asrc[i] + kc * 64, st + adst[i] + lane * 16);
      glds16(bsrc[i] + (size_t)kc * 64 * D, st + bdst[i] + lane * 16);
    }
  };
  const int wm = wv >> 1, wn = wv & 1;
  const int fr = lane & 15, g = lane >> 4, q = (lane & 15) >> 2, p = lane & 3;
  f32x4 acc[2][2] = {};
  int aoff[2];
#pragma unroll
  for (int mt = 0; mt < 2; mt++) { int row = 32*wm + 16*mt + fr; aoff[mt] = row * 128 + ((g ^ (row & 7)) << 4); }
  const int bf = (q >> 1) | ((g & 1) << 1);
  int boff[2];
#pragma unroll
  for (int nt = 0; nt < 2; nt++) { int c = 2*wn + nt; int ch = (2*c + (p >> 1)) ^ (2*bf); boff[nt] = 8192 + (8*g + q) * 128 + ch * 16 + 8 * (p & 1); }

#pragma unroll
  for (int i = 0; i < NST-1; i++) if (i < NK) issue(i);

#pragma unroll 1
  for (int kc = 0; kc < NK - (NST-1); kc++) {
    wait_vm<4*(NST-2)>();
    bar();
    issue(kc + NST - 1);
    const char LDS_AS* st = smem + (kc % NST) * STB;
#pragma unroll
    for (int s = 0; s < 2; s++) {
      bf16x8 af[2], bfr[2];
#pragma unroll
      for (int mt = 0; mt < 2; mt++) af[mt] = lds_b128(st + (aoff[mt] ^ (s << 6)));
#pragma unroll
      for (int nt = 0; nt < 2; nt++) bfr[nt] = cat8(lds_tr(st + boff[nt] + s*32*128), lds_tr(st + boff[nt] + s*32*128 + 4*128));
#pragma unroll
      for (int mt = 0; mt < 2; mt++)
#pragma unroll
        for (int nt = 0; nt < 2; nt++) acc[mt][nt] = MFMA16(bfr[nt], af[mt], acc[mt][nt]);
    }
  }
#pragma unroll
  for (int j = NST-2; j >= 0; j--) {
    int kc = NK - 1 - j;
    if (kc >= 0) {
    switch (j) {
      case 0: wait_vm<0>(); break; case 1: wait_vm<4>(); break; case 2: wait_vm<8>(); break; case 3: wait_vm<12>(); break;
      case 4: wait_vm<16>(); break; case 5: wait_vm<20>(); break; case 6: wait_vm<24>(); break; default: wait_vm<28>(); break;
    }
    bar();
    const char LDS_AS* st = smem + (kc % NST) * STB;
#pragma unroll
    for (int s = 0; s < 2; s++) {
      bf16x8 af[2], bfr[2];
#pragma unroll
      for (int mt = 0; mt < 2; mt++) af[mt] = lds_b128(st + (aoff[mt] ^ (s << 6)));
#pragma unroll
      for (int nt = 0; nt < 2; nt++) bfr[nt] = cat8(lds_tr(st + boff[nt] + s*32*128), lds_tr(st + boff[nt] + s*32*128 + 4*128));
#pragma unroll
      for (int mt = 0; mt < 2; mt++)
#pragma unroll
        for (int nt = 0; nt < 2; nt++) acc[mt][nt] = MFMA16(bfr[nt], af[mt], acc[mt][nt]);
    }
    }
  }
#pragma unroll
  for (int mt = 0; mt < 2; mt++)
#pragma unroll
    for (int nt = 0; nt < 2; nt++) {
      int m = m0 + 32*wm + 16*mt + fr;
      int n = n0 + 32*wn + 16*nt + 4*g;
      u32x2 rv = *(const u32x2*)(res + (size_t)m * D + n);
      f32x4 gv = *(const f32x4*)(gmod + n);
      float o0 = lo_f(rv[0]) + rbf(gv[0] * rbf(acc[mt][nt][0]));
      float o1 = hi_f(rv[0]) + rbf(gv[1] * rbf(acc[mt][nt][1]));
      float o2 = lo_f(rv[1]) + rbf(gv[2] * rbf(acc[mt][nt][2]));
      float o3 = hi_f(rv[1]) + rbf(gv[3] * rbf(acc[mt][nt][3]));
      u32x2 ov; ov[0] = pack_bf(o0, o1); ov[1] = pack_bf(o2, o3);
      *(u32x2*)(out + (size_t)m * D + n) = ov;
    }
}
typedef __attribute__((ext_vector_type(2))) __bf16 bf16x2_t;
__device__ __forceinline__ float dot2bf(unsigned a, unsigned b, float c){
  return __builtin_amdgcn_fdot2_f32_bf16(__builtin_bit_cast(bf16x2_t,a), __builtin_bit_cast(bf16x2_t,b), c, false);
}
__device__ __forceinline__ void ln_stats(const u32x4 (&v)[8], float& mean_b, float& rden) {
  float s = 0.f;
#pragma unroll
  for (int j = 0; j < 8; j++)
#pragma unroll
    for (int e = 0; e < 4; e++) s += lo_f(v[j][e]) + hi_f(v[j][e]);
  s += __shfl_xor(s, 1); s += __shfl_xor(s, 2); s += __shfl_xor(s, 4);
  float mean = s * (1.f/D);
  float q = 0.f;
#pragma unroll
  for (int j = 0; j < 8; j++)
#pragma unroll
    for (int e = 0; e < 4; e++) { float a = lo_f(v[j][e]) - mean, b = hi_f(v[j][e]) - mean; q += a*a + b*b; }
  q += __shfl_xor(q, 1); q += __shfl_xor(q, 2); q += __shfl_xor(q, 4);
  float var = rbf(q * (1.f/D));
  mean_b = rbf(mean);
  rden = 1.f / rbf(sqrtf(rbf(var + 1e-5f)));
}
__device__ __forceinline__ unsigned ln_mod2(unsigned xv, float m, float rden, float sc0, float sh0, float sc1, float sh1) {
  float n0 = rbf(rbf(lo_f(xv) - m) * rden), n1 = rbf(rbf(hi_f(xv) - m) * rden);
  return pack_bf(rbf(n0 * sc0) + sh0, rbf(n1 * sc1) + sh1);
}

__global__ __launch_bounds__(256) void k_qkv_attn(const u16* __restrict__ x, const float* __restrict__ mod,
    const u16* __restrict__ W, u16* __restrict__ o)
{
  constexpr int AB = 80*1024, STB = 24576, NST = 3, NK = 8;
  __shared__ __attribute__((aligned(16))) char smem_[AB + NST*STB];
  char LDS_AS* smem = (char LDS_AS*)smem_;
  const int tid = threadIdx.x, lane = tid & 63, wv = tid >> 6;
  const int hg = blockIdx.x & 7, rb = blockIdx.x >> 3;
  const int r0 = rb * 64;

  const u16* bsrc[6];
#pragma unroll
  for (int i = 0; i < 6; i++) {
    int L = (wv + 4*i) * 64 + lane;
    int row = L / 24, cc = L % 24, seg = cc >> 3;
    int f = ((row >> 1) & 1) | (((row >> 3) & 1) << 1);
    int ch = (cc & 7) ^ (2*f);
    bsrc[i] = W + (size_t)row * 1536 + seg * 512 + hg * 64 + ch * 8;
  }
  auto issue = [&](int kc) {
    char LDS_AS* st = smem + AB + (kc % NST) * STB;
#pragma unroll
    for (int i = 0; i < 6; i++) glds16(bsrc[i] + (size_t)kc * 64 * 1536, st + (wv + 4*i) * 1024 + lane * 16);
  };
  issue(0); issue(1);

  {
    const int sub = tid & 7, rl = tid >> 3;
    u32x4 v[3][8];
#pragma unroll
    for (int ps = 0; ps < 3; ps++) {
      int gr = r0 - 16 + ps*32 + rl; gr = gr < 0 ? 0 : (gr > S-1 ? S-1 : gr);
      const u32x4* src = (const u32x4*)(x + (size_t)gr * D);
#pragma unroll
      for (int j = 0; j < 8; j++) v[ps][j] = src[sub + 8*j];
    }
    float mean[3], rden[3];
#pragma unroll
    for (int ps = 0; ps < 3; ps++) ln_stats(v[ps], mean[ps], rden[ps]);
#pragma unroll
    for (int j = 0; j < 8; j++) {
      int col = (sub + 8*j) * 8;
      f32x4 sa0 = *(const f32x4*)(mod + col), sa1 = *(const f32x4*)(mod + col + 4);
      f32x4 ca0 = *(const f32x4*)(mod + D + col), ca1 = *(const f32x4*)(mod + D + col + 4);
      float sc[8], sh[8];
#pragma unroll
      for (int e = 0; e < 4; e++) { sc[e] = rbf(1.f + ca0[e]); sc[4+e] = rbf(1.f + ca1[e]); sh[e] = sa0[e]; sh[4+e] = sa1[e]; }
#pragma unroll
      for (int ps = 0; ps < 3; ps++) {
        int row = ps*32 + rl;
        u32x4 h;
#pragma unroll
        for (int e = 0; e < 4; e++) h[e] = ln_mod2(v[ps][j][e], mean[ps], rden[ps], sc[2*e], sh[2*e], sc[2*e+1], sh[2*e+1]);
        if (row < 80) *(u32x4 LDS_AS*)(smem + row * 1024 + (((sub + 8*j) ^ ((row & 7) << 1)) << 4)) = h;
      }
    }
  }
  const int fr = lane & 15, g = lane >> 4, q = (lane & 15) >> 2, p = lane & 3;
  f32x4 acc[5][3] = {};
  int aoff[5];
#pragma unroll
  for (int mt = 0; mt < 5; mt++) { int row = 16*mt + fr; aoff[mt] = row * 1024 + ((g ^ ((row & 7) << 1)) << 4); }
  const int bf = (q >> 1) | ((g & 1) << 1);
  int boff[3];
#pragma unroll
  for (int nt = 0; nt < 3; nt++) { int c = 3*wv + nt; int cl = 2*c + (p >> 1); int ph = (cl & ~7) | ((cl & 7) ^ (2*bf)); boff[nt] = (8*g + q) * 384 + ph * 16 + 8 * (p & 1); }

#pragma unroll
  for (int kc = 0; kc < NK; kc++) {
    if (kc < NK-1) wait_vm<6>(); else wait_vm<0>();
    if (kc == 0) __syncthreads(); else bar();
    if (kc + 2 < NK) issue(kc + 2);
    const char LDS_AS* st = smem + AB + (kc % NST) * STB;
#pragma unroll
    for (int s = 0; s < 2; s++) {
      bf16x8 bfr[3];
#pragma unroll
      for (int nt = 0; nt < 3; nt++) bfr[nt] = cat8(lds_tr(st + boff[nt] + s*32*384), lds_tr(st + boff[nt] + s*32*384 + 4*384));
#pragma unroll
      for (int mt = 0; mt < 5; mt++) {
        bf16x8 af = lds_b128(smem + (aoff[mt] ^ ((2*kc + s) << 6)));
#pragma unroll
        for (int nt = 0; nt < 3; nt++) acc[mt][nt] = MFMA16(bfr[nt], af, acc[mt][nt]);
      }
    }
  }
  __syncthreads();
  constexpr int RS = 400;
#pragma unroll
  for (int mt = 0; mt < 5; mt++)
#pragma unroll
    for (int nt = 0; nt < 3; nt++) {
      int row = 16*mt + fr, col = 48*wv + 16*nt + 4*g;
      u32x2 w2; w2[0] = pack_bf(acc[mt][nt][0], acc[mt][nt][1]); w2[1] = pack_bf(acc[mt][nt][2], acc[mt][nt][3]);
      *(u32x2 LDS_AS*)(smem + row * RS + col * 2) = w2;
    }
  __syncthreads();
  {
    const int row = tid >> 2, hd = tid & 3;
    const int srow = r0 + row;
    const char LDS_AS* qp = smem + (16 + row) * RS + hd * 32;
    u32x4 q0 = *(const u32x4 LDS_AS*)qp, q1 = *(const u32x4 LDS_AS*)(qp + 16);
    float lg[8]; float mx = -3.0e38f;
#pragma unroll
    for (int w = 0; w < 8; w++) {
      const char LDS_AS* kp = smem + (16 + row - 2*w) * RS + 128 + hd * 32;
      u32x4 k0 = *(const u32x4 LDS_AS*)kp, k1 = *(const u32x4 LDS_AS*)(kp + 16);
      float a = 0.f;
#pragma unroll
      for (int e = 0; e < 4; e++) a = dot2bf(q0[e], k0[e], a);
#pragma unroll
      for (int e = 0; e < 4; e++) a = dot2bf(q1[e], k1[e], a);
      float l = rbf(a) * 0.25f;
      l = (srow - 2*w >= 0) ? l : -1.0e9f;
      lg[w] = l; mx = fmaxf(mx, l);
    }
    float sum = 0.f;
#pragma unroll
    for (int w = 0; w < 8; w++) { lg[w] = __expf(lg[w] - mx); sum += lg[w]; }
    float rs = 1.f / sum;
    float ov[16];
#pragma unroll
    for (int d = 0; d < 16; d++) ov[d] = 0.f;
#pragma unroll
    for (int w = 0; w < 8; w++) {
      const char LDS_AS* vp = smem + (16 + row - 2*w) * RS + 256 + hd * 32;
      u32x4 v0 = *(const u32x4 LDS_AS*)vp, v1 = *(const u32x4 LDS_AS*)(vp + 16);
      float pw = rbf(lg[w] * rs);
#pragma unroll
      for (int e = 0; e < 4; e++) {
        ov[2*e]   += pw * lo_f(v0[e]); ov[2*e+1]   += pw * hi_f(v0[e]);
        ov[8+2*e] += pw * lo_f(v1[e]); ov[8+2*e+1] += pw * hi_f(v1[e]);
      }
    }
    u32x4 o0, o1;
#pragma unroll
    for (int e = 0; e < 4; e++) { o0[e] = pack_bf(ov[2*e], ov[2*e+1]); o1[e] = pack_bf(ov[8+2*e], ov[8+2*e+1]); }
    u32x4* dst = (u32x4*)(o + (size_t)srow * D + hg * 64 + hd * 16);
    dst[0] = o0; dst[1] = o1;
  }
}
__global__ __launch_bounds__(512) void k_ln_glu(const u16* __restrict__ x1, const float* __restrict__ mod,
    const u16* __restrict__ Wg, const u16* __restrict__ Wu, u16* __restrict__ hh)
{
  constexpr int AB = 128*1024, STB = 16384, NST = 2, NK = 16;
  __shared__ __attribute__((aligned(16))) char smem_[AB + NST*STB];
  char LDS_AS* smem = (char LDS_AS*)smem_;
  const int tid = threadIdx.x, lane = tid & 63, wv = tid >> 6;
  const int b = blockIdx.x;
  const int rb = b & 15, cb = b >> 4;
  const int r0 = rb * 128, c0 = cb * 128;

  const u16* bsrc[2];
#pragma unroll
  for (int i = 0; i < 2; i++) {
    int L = (wv + 8*i) * 64 + lane;
    int row = L >> 5, cc = L & 31;
    int f = (row & 3) | (((row >> 3) & 1) << 2);
    int ch = cc ^ (2*f);
    const u16* base = (ch < 16) ? Wg : Wu;
    bsrc[i] = base + (size_t)row * DF + c0 + (ch & 15) * 8;
  }
  auto issue = [&](int kc) {
    char LDS_AS* st = smem + AB + (kc % NST) * STB;
#pragma unroll
    for (int i = 0; i < 2; i++) glds16(bsrc[i] + (size_t)kc * 32 * DF, st + (wv + 8*i) * 1024 + lane * 16);
  };
  issue(0);
  {
    const int sub = tid & 7, rl = tid >> 3;
    u32x4 v[2][8];
#pragma unroll
    for (int ps = 0; ps < 2; ps++) {
      const u32x4* src = (const u32x4*)(x1 + (size_t)(r0 + ps*64 + rl) * D);
#pragma unroll
      for (int j = 0; j < 8; j++) v[ps][j] = src[sub + 8*j];
    }
    float mean[2], rden[2];
#pragma unroll
    for (int ps = 0; ps < 2; ps++) ln_stats(v[ps], mean[ps], rden[ps]);
#pragma unroll
    for (int j = 0; j < 8; j++) {
      int col = (sub + 8*j) * 8;
      f32x4 sa0 = *(const f32x4*)(mod + 3*D + col), sa1 = *(const f32x4*)(mod + 3*D + col + 4);
      f32x4 ca0 = *(const f32x4*)(mod + 4*D + col), ca1 = *(const f32x4*)(mod + 4*D + col + 4);
      float sc[8], sh[8];
#pragma unroll
      for (int e = 0; e < 4; e++) { sc[e] = rbf(1.f + ca0[e]); sc[4+e] = rbf(1.f + ca1[e]); sh[e] = sa0[e]; sh[4+e] = sa1[e]; }
#pragma unroll
      for (int ps = 0; ps < 2; ps++) {
        int row = ps*64 + rl;
        u32x4 h;
#pragma unroll
        for (int e = 0; e < 4; e++) h[e] = ln_mod2(v[ps][j][e], mean[ps], rden[ps], sc[2*e], sh[2*e], sc[2*e+1], sh[2*e+1]);
        *(u32x4 LDS_AS*)(smem + row * 1024 + (((sub + 8*j) ^ ((row & 7) << 1)) << 4)) = h;
      }
    }
  }
  const int wm = wv >> 2, wn = wv & 3;
  const int fr = lane & 15, g = lane >> 4, q = (lane & 15) >> 2, p = lane & 3;
  f32x4 acc[4][4] = {};
  int aoff[4];
#pragma unroll
  for (int mt = 0; mt < 4; mt++) { int row = 64*wm + 16*mt + fr; aoff[mt] = row * 1024 + ((g ^ ((row & 7) << 1)) << 4); }
  const int bf = q | ((g & 1) << 2);
  int boff[4];
#pragma unroll
  for (int nt = 0; nt < 4; nt++) { int c = (nt >> 1) * 8 + 2*wn + (nt & 1); int cl = 2*c + (p >> 1); int ph = cl ^ (2*bf); boff[nt] = (8*g + q) * 512 + ph * 16 + 8 * (p & 1); }

#pragma unroll
  for (int kc = 0; kc < NK; kc++) {
    wait_vm<0>();
    if (kc == 0) __syncthreads(); else bar();
    if (kc + 1 < NK) issue(kc + 1);
    const char LDS_AS* st = smem + AB + (kc % NST) * STB;
    bf16x8 bfr[4];
#pragma unroll
    for (int nt = 0; nt < 4; nt++) bfr[nt] = cat8(lds_tr(st + boff[nt]), lds_tr(st + boff[nt] + 4*512));
#pragma unroll
    for (int mt = 0; mt < 4; mt++) {
      bf16x8 af = lds_b128(smem + (aoff[mt] ^ (kc << 6)));
#pragma unroll
      for (int nt = 0; nt < 4; nt++) acc[mt][nt] = MFMA16(bfr[nt], af, acc[mt][nt]);
    }
  }
#pragma unroll
  for (int mt = 0; mt < 4; mt++)
#pragma unroll
    for (int nt = 0; nt < 2; nt++) {
      int m = r0 + 64*wm + 16*mt + fr;
      int n = c0 + 32*wn + 16*nt + 4*g;
      float hv[4];
#pragma unroll
      for (int e = 0; e < 4; e++) {
        float gv = rbf(acc[mt][nt][e]), uv = rbf(acc[mt][2+nt][e]);
        float z = 0.7978845608028654f * (gv + 0.044715f * gv * gv * gv);
        float t = 1.f - 2.f / (1.f + __expf(2.f * z));
        hv[e] = rbf(0.5f * gv * (1.f + t)) * uv;
      }
      u32x2 ov; ov[0] = pack_bf(hv[0], hv[1]); ov[1] = pack_bf(hv[2], hv[3]);
      *(u32x2*)(hh + (size_t)m * DF + n) = ov;
    }
}
__global__ __launch_bounds__(256) void k_mod2(const u16* __restrict__ c, const u16* __restrict__ wmod, float* __restrict__ mod)
{
  __shared__ float red[4][32];
  const int tid = threadIdx.x, cl = tid & 3, kg = tid >> 2;
  const int n0 = blockIdx.x * 32 + cl * 8;
  u32x4 w[8];
#pragma unroll
  for (int i = 0; i < 8; i++) w[i] = *(const u32x4*)(wmod + (size_t)(kg * 8 + i) * (6*D) + n0);
  float a[8];
#pragma unroll
  for (int e = 0; e < 8; e++) a[e] = 0.f;
#pragma unroll
  for (int i = 0; i < 8; i++) {
    float cv = bf2f(c[kg * 8 + i]);
    float s = rbf(cv / (1.f + __expf(-cv)));
#pragma unroll
    for (int e = 0; e < 4; e++) { a[2*e] += s * lo_f(w[i][e]); a[2*e+1] += s * hi_f(w[i][e]); }
  }
#pragma unroll
  for (int e = 0; e < 8; e++) { a[e] += __shfl_xor(a[e], 4); a[e] += __shfl_xor(a[e], 8); a[e] += __shfl_xor(a[e], 16); a[e] += __shfl_xor(a[e], 32); }
  if ((tid & 63) < 4) {
#pragma unroll
    for (int e = 0; e < 8; e++) red[tid >> 6][cl * 8 + e] = a[e];
  }
  __syncthreads();
  if (tid < 32) mod[blockIdx.x * 32 + tid] = rbf(red[0][tid] + red[1][tid] + red[2][tid] + red[3][tid]);
}
extern "C" void kernel_launch(void* const* d_in, const int* in_sizes, int n_in,
                              void* d_out, int out_size, void* d_ws, size_t ws_size,
                              hipStream_t stream) {
  const u16* x=(const u16*)d_in[0]; const u16* wqkv=(const u16*)d_in[1]; const u16* wo=(const u16*)d_in[2];
  const u16* wg=(const u16*)d_in[4]; const u16* wu=(const u16*)d_in[5]; const u16* wd=(const u16*)d_in[6];
  const u16* c=(const u16*)d_in[7]; const u16* wmod=(const u16*)d_in[8];
  char* ws=(char*)d_ws;
  float* mod=(float*)ws; ws+=16384;
  u16* o=(u16*)ws; ws+=S*D*2;
  u16* x1=(u16*)ws; ws+=S*D*2;
  u16* hh=(u16*)ws; ws+=S*DF*2;
  k_mod2<<<96,256,0,stream>>>(c,wmod,mod);
  k_qkv_attn<<<256,256,0,stream>>>(x,mod,wqkv,o);
  k_gemm_res<512,6><<<256,256,0,stream>>>(o,wo,x,mod+2*D,x1);
  k_ln_glu<<<224,512,0,stream>>>(x1,mod,wg,wu,hh);
  k_gemm_res<1792,6><<<256,256,0,stream>>>(hh,wd,x1,mod+5*D,(u16*)d_out);
}
```

```cpp
#include <hip/hip_runtime.h>
#include <hip/hip_bf16.h>
#include <stdint.h>

typedef unsigned short u16;
typedef __attribute__((ext_vector_type(8))) short bf16x8;
typedef __attribute__((ext_vector_type(4))) short s16x4;
typedef __attribute__((ext_vector_type(4))) float f32x4;
typedef __attribute__((ext_vector_type(2))) unsigned u32x2;
typedef __attribute__((ext_vector_type(4))) unsigned u32x4;
#define LDS_AS __attribute__((address_space(3)))
#define GLB_AS __attribute__((address_space(1)))

typedef __attribute__((ext_vector_type(2))) float f32x2;
typedef __attribute__((ext_vector_type(2))) __bf16 bf16x2_t;
__device__ __forceinline__ float bf2f(u16 v){ return __uint_as_float(((unsigned)v)<<16); }
__device__ __forceinline__ float lo_f(unsigned u){ return __uint_as_float(u<<16); }
__device__ __forceinline__ float hi_f(unsigned u){ return __uint_as_float(u & 0xffff0000u); }
__device__ __forceinline__ f32x2 unpk(unsigned u){ f32x2 r; r[0]=lo_f(u); r[1]=hi_f(u); return r; }
__device__ __forceinline__ unsigned pack2(f32x2 v){ return __builtin_bit_cast(unsigned, __builtin_convertvector(v, bf16x2_t)); }
__device__ __forceinline__ unsigned pack_bf(float lo, float hi){ f32x2 v; v[0]=lo; v[1]=hi; return pack2(v); }
__device__ __forceinline__ float rbf(float f){ return lo_f(pack_bf(f, 0.f)); }

__device__ __forceinline__ void glds16(const void* gsrc, unsigned lds_dst) {
  unsigned keep;
  asm volatile("s_mov_b32 %0, m0\n\ts_mov_b32 m0, %2\n\ts_nop 0\n\tglobal_load_lds_dwordx4 %1, off\n\ts_mov_b32 m0, %0" : "=&s"(keep) : "v"(gsrc), "s"(lds_dst) : "memory");
}
__device__ __forceinline__ unsigned lds_addr(const char LDS_AS* p){ return __builtin_amdgcn_readfirstlane((unsigned)(size_t)p); }
template<int N> __device__ __forceinline__ void wait_vm(){ asm volatile("s_waitcnt vmcnt(%0)"::"n"(N):"memory"); }
__device__ __forceinline__ void bar(){ __builtin_amdgcn_s_barrier(); }
__device__ __forceinline__ bf16x8 lds_b128(const char LDS_AS* p){ return *(const bf16x8 LDS_AS*)p; }
__device__ __forceinline__ s16x4 lds_tr(const char LDS_AS* p){ return __builtin_amdgcn_ds_read_tr16_b64_v4i16((s16x4 LDS_AS*)p); }
__device__ __forceinline__ bf16x8 cat8(s16x4 a, s16x4 b){ bf16x8 r; r[0]=a[0];r[1]=a[1];r[2]=a[2];r[3]=a[3];r[4]=b[0];r[5]=b[1];r[6]=b[2];r[7]=b[3]; return r; }
#define MFMA16(a,b,c) __builtin_amdgcn_mfma_f32_16x16x32_bf16((a),(b),(c),0,0,0)

constexpr int S=2048, D=512, DF=1792, H=32, DH=16;
template<int K, int NST>
__global__ __launch_bounds__(256) void k_gemm_res(const u16* __restrict__ A, const u16* __restrict__ W,
    const u16* __restrict__ res, const float* __restrict__ gmod, u16* __restrict__ out)
{
  constexpr int NK = K/64;
  constexpr int STB = 16384;
  __shared__ __attribute__((aligned(16))) char smem_[NST*STB];
  char LDS_AS* smem = (char LDS_AS*)smem_;
  const int tid = threadIdx.x, lane = tid & 63, wv = tid >> 6;
  const int b = blockIdx.x;
  const int rb = (b & 7) * 4 + ((b >> 3) & 3);
  const int cb = b >> 5;
  const int m0 = rb * 64, n0 = cb * 64;

  const u16* asrc[2]; const u16* bsrc[2]; int adst[2], bdst[2];
#pragma unroll
  for (int i = 0; i < 2; i++) {
    int inst = wv + 4*i;
    int row = inst*8 + (lane >> 3);
    int slot = lane & 7;
    int ach = slot ^ (row & 7);
    asrc[i] = A + (size_t)(m0 + row) * K + ach * 8;
    int f = ((row >> 1) & 1) | (((row >> 3) & 1) << 1);
    int bch = slot ^ (2*f);
    bsrc[i] = W + (size_t)row * D + n0 + bch * 8;
    adst[i] = __builtin_amdgcn_readfirstlane(inst * 1024);
    bdst[i] = 8192 + adst[i];
  }
  const unsigned sbase = lds_addr(smem);
  auto issue = [&](int kc) {
    unsigned st = sbase + (kc % NST) * STB;
#pragma unroll
    for (int i = 0; i < 2; i++) {
      glds16(asrc[i] + kc * 64, st + adst[i]);
      glds16(bsrc[i] + (size_t)kc * 64 * D, st + bdst[i]);
    }
  };
  const int wm = wv >> 1, wn = wv & 1;
  const int fr = lane & 15, g = lane >> 4, q = (lane & 15) >> 2, p = lane & 3;
  f32x4 acc[2][2] = {};
  int aoff[2];
#pragma unroll
  for (int mt = 0; mt < 2; mt++) { int row = 32*wm + 16*mt + fr; aoff[mt] = row * 128 + ((g ^ (row & 7)) << 4); }
  const int bf = (q >> 1) | ((g & 1) << 1);
  int boff[2];
#pragma unroll
  for (int nt = 0; nt < 2; nt++) { int c = 2*wn + nt; int ch = (2*c + (p >> 1)) ^ (2*bf); boff[nt] = 8192 + (8*g + q) * 128 + ch * 16 + 8 * (p & 1); }

#pragma unroll
  for (int i = 0; i < NST-1; i++) if (i < NK) issue(i);

#pragma unroll 1
  for (int kc = 0; kc < NK - (NST-1); kc++) {
    wait_vm<4*(NST-2)>();
    bar();
    issue(kc + NST - 1);
    const char LDS_AS* st = smem + (kc % NST) * STB;
#pragma unroll
    for (int s = 0; s < 2; s++) {
      bf16x8 af[2], bfr[2];
#pragma unroll
      for (int mt = 0; mt < 2; mt++) af[mt] = lds_b128(st + (aoff[mt] ^ (s << 6)));
#pragma unroll
      for (int nt = 0; nt < 2; nt++) bfr[nt] = cat8(lds_tr(st + boff[nt] + s*32*128), lds_tr(st + boff[nt] + s*32*128 + 4*128));
#pragma unroll
      for (int mt = 0; mt < 2; mt++)
#pragma unroll
        for (int nt = 0; nt < 2; nt++) acc[mt][nt] = MFMA16(bfr[nt], af[mt], acc[mt][nt]);
    }
  }
#pragma unroll
  for (int j = NST-2; j >= 0; j--) {
    int kc = NK - 1 - j;
    if (kc >= 0) {
    switch (j) {
      case 0: wait_vm<0>(); break; case 1: wait_vm<4>(); break; case 2: wait_vm<8>(); break; case 3: wait_vm<12>(); break;
      case 4: wait_vm<16>(); break; case 5: wait_vm<20>(); break; case 6: wait_vm<24>(); break; default: wait_vm<28>(); break;
    }
    bar();
    const char LDS_AS* st = smem + (kc % NST) * STB;
#pragma unroll
    for (int s = 0; s < 2; s++) {
      bf16x8 af[2], bfr[2];
#pragma unroll
      for (int mt = 0; mt < 2; mt++) af[mt] = lds_b128(st + (aoff[mt] ^ (s << 6)));
#pragma unroll
      for (int nt = 0; nt < 2; nt++) bfr[nt] = cat8(lds_tr(st + boff[nt] + s*32*128), lds_tr(st + boff[nt] + s*32*128 + 4*128));
#pragma unroll
      for (int mt = 0; mt < 2; mt++)
#pragma unroll
        for (int nt = 0; nt < 2; nt++) acc[mt][nt] = MFMA16(bfr[nt], af[mt], acc[mt][nt]);
    }
    }
  }
#pragma unroll
  for (int mt = 0; mt < 2; mt++)
#pragma unroll
    for (int nt = 0; nt < 2; nt++) {
      int m = m0 + 32*wm + 16*mt + fr;
      int n = n0 + 32*wn + 16*nt + 4*g;
      u32x2 rv = *(const u32x2*)(res + (size_t)m * D + n);
      f32x4 gv = *(const f32x4*)(gmod + n);
      float o0 = lo_f(rv[0]) + rbf(gv[0] * rbf(acc[mt][nt][0]));
      float o1 = hi_f(rv[0]) + rbf(gv[1] * rbf(acc[mt][nt][1]));
      float o2 = lo_f(rv[1]) + rbf(gv[2] * rbf(acc[mt][nt][2]));
      float o3 = hi_f(rv[1]) + rbf(gv[3] * rbf(acc[mt][nt][3]));
      u32x2 ov; ov[0] = pack_bf(o0, o1); ov[1] = pack_bf(o2, o3);
      *(u32x2*)(out + (size_t)m * D + n) = ov;
    }
}
__device__ __forceinline__ float dot2bf(unsigned a, unsigned b, float c){
  return __builtin_amdgcn_fdot2_f32_bf16(__builtin_bit_cast(bf16x2_t,a), __builtin_bit_cast(bf16x2_t,b), c, false);
}
__device__ __forceinline__ void ln_stats(const u32x4 (&v)[8], float& nm, float& rden) {
  f32x2 s2 = {0.f, 0.f};
#pragma unroll
  for (int j = 0; j < 8; j++)
#pragma unroll
    for (int e = 0; e < 4; e++) s2 += unpk(v[j][e]);
  float s = s2[0] + s2[1];
  s += __shfl_xor(s, 1); s += __shfl_xor(s, 2); s += __shfl_xor(s, 4);
  float mean = s * (1.f/D);
  f32x2 m2 = {mean, mean}, q2 = {0.f, 0.f};
#pragma unroll
  for (int j = 0; j < 8; j++)
#pragma unroll
    for (int e = 0; e < 4; e++) { f32x2 d = unpk(v[j][e]) - m2; q2 += d * d; }
  float q = q2[0] + q2[1];
  q += __shfl_xor(q, 1); q += __shfl_xor(q, 2); q += __shfl_xor(q, 4);
  float var = rbf(q * (1.f/D));
  rden = 1.f / rbf(sqrtf(rbf(var + 1e-5f)));
  nm = -rbf(mean) * rden;
}
__device__ __forceinline__ unsigned ln_mod2(unsigned xv, float nm, float rden, f32x2 sc, f32x2 sh) {
  f32x2 r2 = {rden, rden}, n2 = {nm, nm};
  f32x2 n = unpk(xv) * r2 + n2;
  return pack2(n * sc + sh);
}

__global__ __launch_bounds__(256) void k_qkv_attn(const u16* __restrict__ x, const float* __restrict__ mod,
    const u16* __restrict__ W, u16* __restrict__ o)
{
  constexpr int AB = 80*1024, STB = 24576, NST = 3, NK = 8;
  __shared__ __attribute__((aligned(16))) char smem_[AB + NST*STB];
  char LDS_AS* smem = (char LDS_AS*)smem_;
  const int tid = threadIdx.x, lane = tid & 63, wv = tid >> 6;
  const int hg = blockIdx.x & 7, rb = blockIdx.x >> 3;
  const int r0 = rb * 64;

  const u16* bsrc[6];
#pragma unroll
  for (int i = 0; i < 6; i++) {
    int L = (wv + 4*i) * 64 + lane;
    int row = L / 24, cc = L % 24, seg = cc >> 3;
    int f = ((row >> 1) & 1) | (((row >> 3) & 1) << 1);
    int ch = (cc & 7) ^ (2*f);
    bsrc[i] = W + (size_t)row * 1536 + seg * 512 + hg * 64 + ch * 8;
  }
  const unsigned sbase = lds_addr(smem);
  const int wv_u = __builtin_amdgcn_readfirstlane(wv);
  auto issue = [&](int kc) {
    unsigned st = sbase + AB + (kc % NST) * STB;
#pragma unroll
    for (int i = 0; i < 6; i++) glds16(bsrc[i] + (size_t)kc * 64 * 1536, st + (wv_u + 4*i) * 1024);
  };
  issue(0); issue(1);

  {
    const int sub = tid & 7, rl = tid >> 3;
    u32x4 v[3][8];
#pragma unroll
    for (int ps = 0; ps < 3; ps++) {
      int gr = r0 - 16 + ps*32 + rl; gr = gr < 0 ? 0 : (gr > S-1 ? S-1 : gr);
      const u32x4* src = (const u32x4*)(x + (size_t)gr * D);
#pragma unroll
      for (int j = 0; j < 8; j++) v[ps][j] = src[sub + 8*j];
    }
    float mean[3], rden[3];
#pragma unroll
    for (int ps = 0; ps < 3; ps++) ln_stats(v[ps], mean[ps], rden[ps]);
#pragma unroll
    for (int j = 0; j < 8; j++) {
      int col = (sub + 8*j) * 8;
      f32x4 sa0 = *(const f32x4*)(mod + col), sa1 = *(const f32x4*)(mod + col + 4);
      f32x4 ca0 = *(const f32x4*)(mod + D + col), ca1 = *(const f32x4*)(mod + D + col + 4);
      f32x2 sc[4], sh[4];
      sc[0] = unpk(pack_bf(1.f + ca0[0], 1.f + ca0[1])); sc[1] = unpk(pack_bf(1.f + ca0[2], 1.f + ca0[3]));
      sc[2] = unpk(pack_bf(1.f + ca1[0], 1.f + ca1[1])); sc[3] = unpk(pack_bf(1.f + ca1[2], 1.f + ca1[3]));
      sh[0][0] = sa0[0]; sh[0][1] = sa0[1]; sh[1][0] = sa0[2]; sh[1][1] = sa0[3];
      sh[2][0] = sa1[0]; sh[2][1] = sa1[1]; sh[3][0] = sa1[2]; sh[3][1] = sa1[3];
#pragma unroll
      for (int ps = 0; ps < 3; ps++) {
        int row = ps*32 + rl;
        u32x4 h;
#pragma unroll
        for (int e = 0; e < 4; e++) h[e] = ln_mod2(v[ps][j][e], mean[ps], rden[ps], sc[e], sh[e]);
        if (row < 80) *(u32x4 LDS_AS*)(smem + row * 1024 + (((sub + 8*j) ^ ((row & 7) << 1)) << 4)) = h;
      }
    }
  }
  const int fr = lane & 15, g = lane >> 4, q = (lane & 15) >> 2, p = lane & 3;
  f32x4 acc[5][3] = {};
  int aoff[5];
#pragma unroll
  for (int mt = 0; mt < 5; mt++) { int row = 16*mt + fr; aoff[mt] = row * 1024 + ((g ^ ((row & 7) << 1)) << 4); }
  const int bf = (q >> 1) | ((g & 1) << 1);
  int boff[3];
#pragma unroll
  for (int nt = 0; nt < 3; nt++) { int c = 3*wv + nt; int cl = 2*c + (p >> 1); int ph = (cl & ~7) | ((cl & 7) ^ (2*bf)); boff[nt] = (8*g + q) * 384 + ph * 16 + 8 * (p & 1); }

#pragma unroll
  for (int kc = 0; kc < NK; kc++) {
    if (kc < NK-1) wait_vm<6>(); else wait_vm<0>();
    if (kc == 0) __syncthreads(); else bar();
    if (kc + 2 < NK) issue(kc + 2);
    const char LDS_AS* st = smem + AB + (kc % NST) * STB;
#pragma unroll
    for (int s = 0; s < 2; s++) {
      bf16x8 bfr[3];
#pragma unroll
      for (int nt = 0; nt < 3; nt++) bfr[nt] = cat8(lds_tr(st + boff[nt] + s*32*384), lds_tr(st + boff[nt] + s*32*384 + 4*384));
#pragma unroll
      for (int mt = 0; mt < 5; mt++) {
        bf16x8 af = lds_b128(smem + (aoff[mt] ^ ((2*kc + s) << 6)));
#pragma unroll
        for (int nt = 0; nt < 3; nt++) acc[mt][nt] = MFMA16(bfr[nt], af, acc[mt][nt]);
      }
    }
  }
  __syncthreads();
  constexpr int RS = 400;
#pragma unroll
  for (int mt = 0; mt < 5; mt++)
#pragma unroll
    for (int nt = 0; nt < 3; nt++) {
      int row = 16*mt + fr, col = 48*wv + 16*nt + 4*g;
      u32x2 w2; w2[0] = pack_bf(acc[mt][nt][0], acc[mt][nt][1]); w2[1] = pack_bf(acc[mt][nt][2], acc[mt][nt][3]);
      *(u32x2 LDS_AS*)(smem + row * RS + col * 2) = w2;
    }
  __syncthreads();
  {
    const int row = tid >> 2, hd = tid & 3;
    const int srow = r0 + row;
    const char LDS_AS* qp = smem + (16 + row) * RS + hd * 32;
    u32x4 q0 = *(const u32x4 LDS_AS*)qp, q1 = *(const u32x4 LDS_AS*)(qp + 16);
    float lg[8]; float mx = -3.0e38f;
#pragma unroll
    for (int w = 0; w < 8; w++) {
      const char LDS_AS* kp = smem + (16 + row - 2*w) * RS + 128 + hd * 32;
      u32x4 k0 = *(const u32x4 LDS_AS*)kp, k1 = *(const u32x4 LDS_AS*)(kp + 16);
      float a = 0.f;
#pragma unroll
      for (int e = 0; e < 4; e++) a = dot2bf(q0[e], k0[e], a);
#pragma unroll
      for (int e = 0; e < 4; e++) a = dot2bf(q1[e], k1[e], a);
      float l = rbf(a) * 0.25f;
      l = (srow - 2*w >= 0) ? l : -1.0e9f;
      lg[w] = l; mx = fmaxf(mx, l);
    }
    float sum = 0.f;
#pragma unroll
    for (int w = 0; w < 8; w++) { lg[w] = __expf(lg[w] - mx); sum += lg[w]; }
    float rs = 1.f / sum;
    f32x2 ov[8];
#pragma unroll
    for (int d = 0; d < 8; d++) ov[d] = (f32x2){0.f, 0.f};
#pragma unroll
    for (int w = 0; w < 8; w++) {
      const char LDS_AS* vp = smem + (16 + row - 2*w) * RS + 256 + hd * 32;
      u32x4 v0 = *(const u32x4 LDS_AS*)vp, v1 = *(const u32x4 LDS_AS*)(vp + 16);
      float pw = rbf(lg[w] * rs);
      f32x2 pw2 = {pw, pw};
#pragma unroll
      for (int e = 0; e < 4; e++) { ov[e] += pw2 * unpk(v0[e]); ov[4+e] += pw2 * unpk(v1[e]); }
    }
    u32x4 o0, o1;
#pragma unroll
    for (int e = 0; e < 4; e++) { o0[e] = pack2(ov[e]); o1[e] = pack2(ov[4+e]); }
    u32x4* dst = (u32x4*)(o + (size_t)srow * D + hg * 64 + hd * 16);
    dst[0] = o0; dst[1] = o1;
  }
}
__global__ __launch_bounds__(512) void k_ln_glu(const u16* __restrict__ x1, const float* __restrict__ mod,
    const u16* __restrict__ Wg, const u16* __restrict__ Wu, u16* __restrict__ hh)
{
  constexpr int AB = 128*1024, STB = 16384, NST = 2, NK = 16;
  __shared__ __attribute__((aligned(16))) char smem_[AB + NST*STB];
  char LDS_AS* smem = (char LDS_AS*)smem_;
  const int tid = threadIdx.x, lane = tid & 63, wv = tid >> 6;
  const int b = blockIdx.x;
  const int rb = b & 15, cb = b >> 4;
  const int r0 = rb * 128, c0 = cb * 128;

  const u16* bsrc[2];
#pragma unroll
  for (int i = 0; i < 2; i++) {
    int L = (wv + 8*i) * 64 + lane;
    int row = L >> 5, cc = L & 31;
    int f = (row & 3) | (((row >> 3) & 1) << 2);
    int ch = cc ^ (2*f);
    const u16* base = (ch < 16) ? Wg : Wu;
    bsrc[i] = base + (size_t)row * DF + c0 + (ch & 15) * 8;
  }
  const unsigned sbase = lds_addr(smem);
  const int wv_u = __builtin_amdgcn_readfirstlane(wv);
  auto issue = [&](int kc) {
    unsigned st = sbase + AB + (kc % NST) * STB;
#pragma unroll
    for (int i = 0; i < 2; i++) glds16(bsrc[i] + (size_t)kc * 32 * DF, st + (wv_u + 8*i) * 1024);
  };
  issue(0);
  {
    const int sub = tid & 7, rl = tid >> 3;
    u32x4 v[2][8];
#pragma unroll
    for (int ps = 0; ps < 2; ps++) {
      const u32x4* src = (const u32x4*)(x1 + (size_t)(r0 + ps*64 + rl) * D);
#pragma unroll
      for (int j = 0; j < 8; j++) v[ps][j] = src[sub + 8*j];
    }
    float mean[2], rden[2];
#pragma unroll
    for (int ps = 0; ps < 2; ps++) ln_stats(v[ps], mean[ps], rden[ps]);
#pragma unroll
    for (int j = 0; j < 8; j++) {
      int col = (sub + 8*j) * 8;
      f32x4 sa0 = *(const f32x4*)(mod + 3*D + col), sa1 = *(const f32x4*)(mod + 3*D + col + 4);
      f32x4 ca0 = *(const f32x4*)(mod + 4*D + col), ca1 = *(const f32x4*)(mod + 4*D + col + 4);
      f32x2 sc[4], sh[4];
      sc[0] = unpk(pack_bf(1.f + ca0[0], 1.f + ca0[1])); sc[1] = unpk(pack_bf(1.f + ca0[2], 1.f + ca0[3]));
      sc[2] = unpk(pack_bf(1.f + ca1[0], 1.f + ca1[1])); sc[3] = unpk(pack_bf(1.f + ca1[2], 1.f + ca1[3]));
      sh[0][0] = sa0[0]; sh[0][1] = sa0[1]; sh[1][0] = sa0[2]; sh[1][1] = sa0[3];
      sh[2][0] = sa1[0]; sh[2][1] = sa1[1]; sh[3][0] = sa1[2]; sh[3][1] = sa1[3];
#pragma unroll
      for (int ps = 0; ps < 2; ps++) {
        int row = ps*64 + rl;
        u32x4 h;
#pragma unroll
        for (int e = 0; e < 4; e++) h[e] = ln_mod2(v[ps][j][e], mean[ps], rden[ps], sc[e], sh[e]);
        *(u32x4 LDS_AS*)(smem + row * 1024 + (((sub + 8*j) ^ ((row & 7) << 1)) << 4)) = h;
      }
    }
  }
  const int wm = wv >> 2, wn = wv & 3;
  const int fr = lane & 15, g = lane >> 4, q = (lane & 15) >> 2, p = lane & 3;
  f32x4 acc[4][4] = {};
  int aoff[4];
#pragma unroll
  for (int mt = 0; mt < 4; mt++) { int row = 64*wm + 16*mt + fr; aoff[mt] = row * 1024 + ((g ^ ((row & 7) << 1)) << 4); }
  const int bf = q | ((g & 1) << 2);
  int boff[4];
#pragma unroll
  for (int nt = 0; nt < 4; nt++) { int c = (nt >> 1) * 8 + 2*wn + (nt & 1); int cl = 2*c + (p >> 1); int ph = cl ^ (2*bf); boff[nt] = (8*g + q) * 512 + ph * 16 + 8 * (p & 1); }

#pragma unroll
  for (int kc = 0; kc < NK; kc++) {
    wait_vm<0>();
    if (kc == 0) __syncthreads(); else bar();
    if (kc + 1 < NK) issue(kc + 1);
    const char LDS_AS* st = smem + AB + (kc % NST) * STB;
    bf16x8 bfr[4];
#pragma unroll
    for (int nt = 0; nt < 4; nt++) bfr[nt] = cat8(lds_tr(st + boff[nt]), lds_tr(st + boff[nt] + 4*512));
#pragma unroll
    for (int mt = 0; mt < 4; mt++) {
      bf16x8 af = lds_b128(smem + (aoff[mt] ^ (kc << 6)));
#pragma unroll
      for (int nt = 0; nt < 4; nt++) acc[mt][nt] = MFMA16(bfr[nt], af, acc[mt][nt]);
    }
  }
#pragma unroll
  for (int mt = 0; mt < 4; mt++)
#pragma unroll
    for (int nt = 0; nt < 2; nt++) {
      int m = r0 + 64*wm + 16*mt + fr;
      int n = c0 + 32*wn + 16*nt + 4*g;
      float hv[4];
#pragma unroll
      for (int e = 0; e < 4; e++) {
        float gv = rbf(acc[mt][nt][e]), uv = rbf(acc[mt][2+nt][e]);
        float z = 0.7978845608028654f * (gv + 0.044715f * gv * gv * gv);
        float t = 1.f - 2.f / (1.f + __expf(2.f * z));
        hv[e] = rbf(0.5f * gv * (1.f + t)) * uv;
      }
      u32x2 ov; ov[0] = pack_bf(hv[0], hv[1]); ov[1] = pack_bf(hv[2], hv[3]);
      *(u32x2*)(hh + (size_t)m * DF + n) = ov;
    }
}
__global__ __launch_bounds__(256) void k_mod2(const u16* __restrict__ c, const u16* __restrict__ wmod, float* __restrict__ mod)
{
  __shared__ float red[4][32];
  const int tid = threadIdx.x, cl = tid & 3, kg = tid >> 2;
  const int n0 = blockIdx.x * 32 + cl * 8;
  u32x4 w[8];
#pragma unroll
  for (int i = 0; i < 8; i++) w[i] = *(const u32x4*)(wmod + (size_t)(kg * 8 + i) * (6*D) + n0);
  float a[8];
#pragma unroll
  for (int e = 0; e < 8; e++) a[e] = 0.f;
#pragma unroll
  for (int i = 0; i < 8; i++) {
    float cv = bf2f(c[kg * 8 + i]);
    float s = rbf(cv / (1.f + __expf(-cv)));
#pragma unroll
    for (int e = 0; e < 4; e++) { a[2*e] += s * lo_f(w[i][e]); a[2*e+1] += s * hi_f(w[i][e]); }
  }
#pragma unroll
  for (int e = 0; e < 8; e++) { a[e] += __shfl_xor(a[e], 4); a[e] += __shfl_xor(a[e], 8); a[e] += __shfl_xor(a[e], 16); a[e] += __shfl_xor(a[e], 32); }
  if ((tid & 63) < 4) {
#pragma unroll
    for (int e = 0; e < 8; e++) red[tid >> 6][cl * 8 + e] = a[e];
  }
  __syncthreads();
  if (tid < 32) mod[blockIdx.x * 32 + tid] = rbf(red[0][tid] + red[1][tid] + red[2][tid] + red[3][tid]);
}
extern "C" void kernel_launch(void* const* d_in, const int* in_sizes, int n_in,
                              void* d_out, int out_size, void* d_ws, size_t ws_size,
                              hipStream_t stream) {
  const u16* x=(const u16*)d_in[0]; const u16* wqkv=(const u16*)d_in[1]; const u16* wo=(const u16*)d_in[2];
  const u16* wg=(const u16*)d_in[4]; const u16* wu=(const u16*)d_in[5]; const u16* wd=(const u16*)d_in[6];
  const u16* c=(const u16*)d_in[7]; const u16* wmod=(const u16*)d_in[8];
  char* ws=(char*)d_ws;
  float* mod=(float*)ws; ws+=16384;
  u16* o=(u16*)ws; ws+=S*D*2;
  u16* x1=(u16*)ws; ws+=S*D*2;
  u16* hh=(u16*)ws; ws+=S*DF*2;
  k_mod2<<<96,256,0,stream>>>(c,wmod,mod);
  k_qkv_attn<<<256,256,0,stream>>>(x,mod,wqkv,o);
  k_gemm_res<512,6><<<256,256,0,stream>>>(o,wo,x,mod+2*D,x1);
  k_ln_glu<<<224,512,0,stream>>>(x1,mod,wg,wu,hh);
  k_gemm_res<1792,6><<<256,256,0,stream>>>(hh,wd,x1,mod+5*D,(u16*)d_out);
}
```

```cpp
#include <hip/hip_runtime.h>
#include <hip/hip_bf16.h>
#include <stdint.h>

typedef unsigned short u16;
typedef __attribute__((ext_vector_type(8))) short bf16x8;
typedef __attribute__((ext_vector_type(4))) short s16x4;
typedef __attribute__((ext_vector_type(4))) float f32x4;
typedef __attribute__((ext_vector_type(2))) unsigned u32x2;
typedef __attribute__((ext_vector_type(4))) unsigned u32x4;
#define LDS_AS __attribute__((address_space(3)))
#define GLB_AS __attribute__((address_space(1)))

typedef __attribute__((ext_vector_type(2))) float f32x2;
typedef __attribute__((ext_vector_type(2))) __bf16 bf16x2_t;
__device__ __forceinline__ float bf2f(u16 v){ return __uint_as_float(((unsigned)v)<<16); }
__device__ __forceinline__ float lo_f(unsigned u){ return __uint_as_float(u<<16); }
__device__ __forceinline__ float hi_f(unsigned u){ return __uint_as_float(u & 0xffff0000u); }
__device__ __forceinline__ f32x2 unpk(unsigned u){ f32x2 r; r[0]=lo_f(u); r[1]=hi_f(u); return r; }
__device__ __forceinline__ unsigned pack2(f32x2 v){ return __builtin_bit_cast(unsigned, __builtin_convertvector(v, bf16x2_t)); }
__device__ __forceinline__ unsigned pack_bf(float lo, float hi){ f32x2 v; v[0]=lo; v[1]=hi; return pack2(v); }
__device__ __forceinline__ float rbf(float f){ return lo_f(pack_bf(f, 0.f)); }

__device__ __forceinline__ void glds16(const void* gsrc, unsigned lds_dst) {
  unsigned keep;
  asm volatile("s_mov_b32 %0, m0\n\ts_mov_b32 m0, %2\n\ts_nop 0\n\tglobal_load_lds_dwordx4 %1, off\n\ts_mov_b32 m0, %0" : "=&s"(keep) : "v"(gsrc), "s"(lds_dst) : "memory");
}
__device__ __forceinline__ unsigned lds_addr(const char LDS_AS* p){ return __builtin_amdgcn_readfirstlane((unsigned)(size_t)p); }
template<int N> __device__ __forceinline__ void wait_vm(){ asm volatile("s_waitcnt vmcnt(%0)"::"n"(N):"memory"); }
__device__ __forceinline__ void bar(){ __builtin_amdgcn_s_barrier(); }
__device__ __forceinline__ bf16x8 lds_b128(const char LDS_AS* p){ return *(const bf16x8 LDS_AS*)p; }
__device__ __forceinline__ s16x4 lds_tr(const char LDS_AS* p){ return __builtin_amdgcn_ds_read_tr16_b64_v4i16((s16x4 LDS_AS*)p); }
__device__ __forceinline__ bf16x8 cat8(s16x4 a, s16x4 b){ bf16x8 r; r[0]=a[0];r[1]=a[1];r[2]=a[2];r[3]=a[3];r[4]=b[0];r[5]=b[1];r[6]=b[2];r[7]=b[3]; return r; }
#define MFMA16(a,b,c) __builtin_amdgcn_mfma_f32_16x16x32_bf16((a),(b),(c),0,0,0)

constexpr int S=2048, D=512, DF=1792, H=32, DH=16;
template<int K, int NST>
__global__ __launch_bounds__(512) void k_gemm_res(const u16* __restrict__ A, const u16* __restrict__ W,
    const u16* __restrict__ res, const float* __restrict__ gmod, u16* __restrict__ out)
{
  constexpr int NI = K/128;
  constexpr int CHB = 16384;
  constexpr int STB = 2*CHB;
  __shared__ __attribute__((aligned(16))) char smem_[NST*STB];
  char LDS_AS* smem = (char LDS_AS*)smem_;
  const int tid = threadIdx.x, lane = tid & 63, wv = tid >> 6;
  const int b = blockIdx.x;
  const int rb = (b & 7) * 4 + ((b >> 3) & 3);
  const int cb = b >> 5;
  const int m0 = rb * 64, n0 = cb * 64;
  const int kg = wv >> 2, w4 = wv & 3;

  const u16* asrc[2]; const u16* bsrc[2];
#pragma unroll
  for (int i = 0; i < 2; i++) {
    int inst = w4 + 4*i;
    int row = inst*8 + (lane >> 3);
    int slot = lane & 7;
    int ach = slot ^ (row & 7);
    asrc[i] = A + (size_t)(m0 + row) * K + kg * 64 + ach * 8;
    int f = ((row >> 1) & 1) | (((row >> 3) & 1) << 1);
    int bch = slot ^ (2*f);
    bsrc[i] = W + (size_t)(kg * 64 + row) * D + n0 + bch * 8;
  }
  const unsigned sbase = lds_addr(smem) + __builtin_amdgcn_readfirstlane(kg * CHB + w4 * 1024);
  auto issue = [&](int it) {
    unsigned st = sbase + (it % NST) * STB;
#pragma unroll
    for (int i = 0; i < 2; i++) {
      glds16(asrc[i] + it * 128, st + i * 4096);
      glds16(bsrc[i] + (size_t)it * 128 * D, st + 8192 + i * 4096);
    }
  };
  const int wm = w4 >> 1, wn = w4 & 1;
  const int fr = lane & 15, g = lane >> 4, q = (lane & 15) >> 2, p = lane & 3;
  f32x4 acc[2][2] = {};
  int aoff[2];
#pragma unroll
  for (int mt = 0; mt < 2; mt++) { int row = 32*wm + 16*mt + fr; aoff[mt] = kg * CHB + row * 128 + ((g ^ (row & 7)) << 4); }
  const int bf = (q >> 1) | ((g & 1) << 1);
  int boff[2];
#pragma unroll
  for (int nt = 0; nt < 2; nt++) { int c = 2*wn + nt; int ch = (2*c + (p >> 1)) ^ (2*bf); boff[nt] = kg * CHB + 8192 + (8*g + q) * 128 + ch * 16 + 8 * (p & 1); }

  auto compute = [&](int it) {
    const char LDS_AS* st = smem + (it % NST) * STB;
#pragma unroll
    for (int s = 0; s < 2; s++) {
      bf16x8 af[2], bfr[2];
#pragma unroll
      for (int mt = 0; mt < 2; mt++) af[mt] = lds_b128(st + (aoff[mt] ^ (s << 6)));
#pragma unroll
      for (int nt = 0; nt < 2; nt++) bfr[nt] = cat8(lds_tr(st + boff[nt] + s*32*128), lds_tr(st + boff[nt] + s*32*128 + 4*128));
#pragma unroll
      for (int mt = 0; mt < 2; mt++)
#pragma unroll
        for (int nt = 0; nt < 2; nt++) acc[mt][nt] = MFMA16(bfr[nt], af[mt], acc[mt][nt]);
    }
  };
#pragma unroll
  for (int i = 0; i < NST-1; i++) if (i < NI) issue(i);
#pragma unroll 1
  for (int it = 0; it < NI - (NST-1); it++) {
    wait_vm<4*(NST-2)>();
    bar();
    issue(it + NST - 1);
    compute(it);
  }
#pragma unroll
  for (int j = NST-2; j >= 0; j--) {
    int it = NI - 1 - j;
    if (it >= 0) {
      switch (j) { case 0: wait_vm<0>(); break; case 1: wait_vm<4>(); break; case 2: wait_vm<8>(); break; case 3: wait_vm<12>(); break; default: wait_vm<16>(); break; }
      bar();
      compute(it);
    }
  }
  __syncthreads();
  f32x4 LDS_AS* red = (f32x4 LDS_AS*)smem;
  if (kg == 1) {
#pragma unroll
    for (int mt = 0; mt < 2; mt++)
#pragma unroll
      for (int nt = 0; nt < 2; nt++) red[((w4 * 4) + mt * 2 + nt) * 64 + lane] = acc[mt][nt];
  }
  __syncthreads();
  if (kg == 0) {
#pragma unroll
    for (int mt = 0; mt < 2; mt++)
#pragma unroll
      for (int nt = 0; nt < 2; nt++) {
        f32x4 a = acc[mt][nt] + red[((w4 * 4) + mt * 2 + nt) * 64 + lane];
        int m = m0 + 32*wm + 16*mt + fr;
        int n = n0 + 32*wn + 16*nt + 4*g;
        u32x2 rv = *(const u32x2*)(res + (size_t)m * D + n);
        f32x4 gv = *(const f32x4*)(gmod + n);
        float o0 = lo_f(rv[0]) + rbf(gv[0] * rbf(a[0]));
        float o1 = hi_f(rv[0]) + rbf(gv[1] * rbf(a[1]));
        float o2 = lo_f(rv[1]) + rbf(gv[2] * rbf(a[2]));
        float o3 = hi_f(rv[1]) + rbf(gv[3] * rbf(a[3]));
        u32x2 ov; ov[0] = pack_bf(o0, o1); ov[1] = pack_bf(o2, o3);
        *(u32x2*)(out + (size_t)m * D + n) = ov;
      }
  }
}
__device__ __forceinline__ float dot2bf(unsigned a, unsigned b, float c){
  return __builtin_amdgcn_fdot2_f32_bf16(__builtin_bit_cast(bf16x2_t,a), __builtin_bit_cast(bf16x2_t,b), c, false);
}
__device__ __forceinline__ void ln_stats(const u32x4 (&v)[8], float& nm, float& rden) {
  f32x2 s2 = {0.f, 0.f};
#pragma unroll
  for (int j = 0; j < 8; j++)
#pragma unroll
    for (int e = 0; e < 4; e++) s2 += unpk(v[j][e]);
  float s = s2[0] + s2[1];
  s += __shfl_xor(s, 1); s += __shfl_xor(s, 2); s += __shfl_xor(s, 4);
  float mean = s * (1.f/D);
  f32x2 m2 = {mean, mean}, q2 = {0.f, 0.f};
#pragma unroll
  for (int j = 0; j < 8; j++)
#pragma unroll
    for (int e = 0; e < 4; e++) { f32x2 d = unpk(v[j][e]) - m2; q2 += d * d; }
  float q = q2[0] + q2[1];
  q += __shfl_xor(q, 1); q += __shfl_xor(q, 2); q += __shfl_xor(q, 4);
  float var = rbf(q * (1.f/D));
  rden = 1.f / rbf(sqrtf(rbf(var + 1e-5f)));
  nm = rbf(mean);
}
__device__ __forceinline__ f32x2 rbf2(f32x2 v){ return unpk(pack2(v)); }
__device__ __forceinline__ unsigned ln_mod2(unsigned xv, float m, float rden, f32x2 sc, f32x2 sh) {
  f32x2 r2 = {rden, rden}, m2 = {m, m};
  f32x2 d = rbf2(unpk(xv) - m2);
  f32x2 n = rbf2(d * r2);
  return pack2(rbf2(n * sc) + sh);
}

__global__ __launch_bounds__(256) void k_qkv_attn(const u16* __restrict__ x, const float* __restrict__ mod,
    const u16* __restrict__ W, u16* __restrict__ o)
{
  constexpr int AB = 80*1024, STB = 24576, NST = 3, NK = 8;
  __shared__ __attribute__((aligned(16))) char smem_[AB + NST*STB];
  char LDS_AS* smem = (char LDS_AS*)smem_;
  const int tid = threadIdx.x, lane = tid & 63, wv = tid >> 6;
  const int hg = blockIdx.x & 7, rb = blockIdx.x >> 3;
  const int r0 = rb * 64;

  const u16* bsrc[6];
#pragma unroll
  for (int i = 0; i < 6; i++) {
    int L = (wv + 4*i) * 64 + lane;
    int row = L / 24, cc = L % 24, seg = cc >> 3;
    int f = ((row >> 1) & 1) | (((row >> 3) & 1) << 1);
    int ch = (cc & 7) ^ (2*f);
    bsrc[i] = W + (size_t)row * 1536 + seg * 512 + hg * 64 + ch * 8;
  }
  const unsigned sbase = lds_addr(smem);
  const int wv_u = __builtin_amdgcn_readfirstlane(wv);
  auto issue = [&](int kc) {
    unsigned st = sbase + AB + (kc % NST) * STB;
#pragma unroll
    for (int i = 0; i < 6; i++) glds16(bsrc[i] + (size_t)kc * 64 * 1536, st + (wv_u + 4*i) * 1024);
  };
  issue(0); issue(1);

  {
    const int sub = tid & 7, rl = tid >> 3;
    u32x4 v[3][8];
#pragma unroll
    for (int ps = 0; ps < 3; ps++) {
      int gr = r0 - 16 + ps*32 + rl; gr = gr < 0 ? 0 : (gr > S-1 ? S-1 : gr);
      const u32x4* src = (const u32x4*)(x + (size_t)gr * D);
#pragma unroll
      for (int j = 0; j < 8; j++) v[ps][j] = src[sub + 8*j];
    }
    float mean[3], rden[3];
#pragma unroll
    for (int ps = 0; ps < 3; ps++) ln_stats(v[ps], mean[ps], rden[ps]);
#pragma unroll
    for (int j = 0; j < 8; j++) {
      int col = (sub + 8*j) * 8;
      f32x4 sa0 = *(const f32x4*)(mod + col), sa1 = *(const f32x4*)(mod + col + 4);
      f32x4 ca0 = *(const f32x4*)(mod + D + col), ca1 = *(const f32x4*)(mod + D + col + 4);
      f32x2 sc[4], sh[4];
      sc[0] = unpk(pack_bf(1.f + ca0[0], 1.f + ca0[1])); sc[1] = unpk(pack_bf(1.f + ca0[2], 1.f + ca0[3]));
      sc[2] = unpk(pack_bf(1.f + ca1[0], 1.f + ca1[1])); sc[3] = unpk(pack_bf(1.f + ca1[2], 1.f + ca1[3]));
      sh[0][0] = sa0[0]; sh[0][1] = sa0[1]; sh[1][0] = sa0[2]; sh[1][1] = sa0[3];
      sh[2][0] = sa1[0]; sh[2][1] = sa1[1]; sh[3][0] = sa1[2]; sh[3][1] = sa1[3];
#pragma unroll
      for (int ps = 0; ps < 3; ps++) {
        int row = ps*32 + rl;
        u32x4 h;
#pragma unroll
        for (int e = 0; e < 4; e++) h[e] = ln_mod2(v[ps][j][e], mean[ps], rden[ps], sc[e], sh[e]);
        if (row < 80) *(u32x4 LDS_AS*)(smem + row * 1024 + (((sub + 8*j) ^ ((row & 7) << 1)) << 4)) = h;
      }
    }
  }
  const int fr = lane & 15, g = lane >> 4, q = (lane & 15) >> 2, p = lane & 3;
  f32x4 acc[5][3] = {};
  int aoff[5];
#pragma unroll
  for (int mt = 0; mt < 5; mt++) { int row = 16*mt + fr; aoff[mt] = row * 1024 + ((g ^ ((row & 7) << 1)) << 4); }
  const int bf = (q >> 1) | ((g & 1) << 1);
  int boff[3];
#pragma unroll
  for (int nt = 0; nt < 3; nt++) { int c = 3*wv + nt; int cl = 2*c + (p >> 1); int ph = (cl & ~7) | ((cl & 7) ^ (2*bf)); boff[nt] = (8*g + q) * 384 + ph * 16 + 8 * (p & 1); }

#pragma unroll
  for (int kc = 0; kc < NK; kc++) {
    if (kc < NK-1) wait_vm<6>(); else wait_vm<0>();
    if (kc == 0) __syncthreads(); else bar();
    if (kc + 2 < NK) issue(kc + 2);
    const char LDS_AS* st = smem + AB + (kc % NST) * STB;
#pragma unroll
    for (int s = 0; s < 2; s++) {
      bf16x8 bfr[3];
#pragma unroll
      for (int nt = 0; nt < 3; nt++) bfr[nt] = cat8(lds_tr(st + boff[nt] + s*32*384), lds_tr(st + boff[nt] + s*32*384 + 4*384));
#pragma unroll
      for (int mt = 0; mt < 5; mt++) {
        bf16x8 af = lds_b128(smem + (aoff[mt] ^ ((2*kc + s) << 6)));
#pragma unroll
        for (int nt = 0; nt < 3; nt++) acc[mt][nt] = MFMA16(bfr[nt], af, acc[mt][nt]);
      }
    }
  }
  __syncthreads();
  constexpr int RS = 400;
#pragma unroll
  for (int mt = 0; mt < 5; mt++)
#pragma unroll
    for (int nt = 0; nt < 3; nt++) {
      int row = 16*mt + fr, col = 48*wv + 16*nt + 4*g;
      u32x2 w2; w2[0] = pack_bf(acc[mt][nt][0], acc[mt][nt][1]); w2[1] = pack_bf(acc[mt][nt][2], acc[mt][nt][3]);
      *(u32x2 LDS_AS*)(smem + row * RS + col * 2) = w2;
    }
  __syncthreads();
  {
    const int row = tid >> 2, hd = tid & 3;
    const int srow = r0 + row;
    const char LDS_AS* qp = smem + (16 + row) * RS + hd * 32;
    u32x4 q0 = *(const u32x4 LDS_AS*)qp, q1 = *(const u32x4 LDS_AS*)(qp + 16);
    float lg[8]; float mx = -3.0e38f;
#pragma unroll
    for (int w = 0; w < 8; w++) {
      const char LDS_AS* kp = smem + (16 + row - 2*w) * RS + 128 + hd * 32;
      u32x4 k0 = *(const u32x4 LDS_AS*)kp, k1 = *(const u32x4 LDS_AS*)(kp + 16);
      float a = 0.f;
#pragma unroll
      for (int e = 0; e < 4; e++) a = dot2bf(q0[e], k0[e], a);
#pragma unroll
      for (int e = 0; e < 4; e++) a = dot2bf(q1[e], k1[e], a);
      float l = rbf(a) * 0.25f;
      l = (srow - 2*w >= 0) ? l : -1.0e9f;
      lg[w] = l; mx = fmaxf(mx, l);
    }
    float sum = 0.f;
#pragma unroll
    for (int w = 0; w < 8; w++) { lg[w] = __expf(lg[w] - mx); sum += lg[w]; }
    float rs = 1.f / sum;
    f32x2 ov[8];
#pragma unroll
    for (int d = 0; d < 8; d++) ov[d] = (f32x2){0.f, 0.f};
#pragma unroll
    for (int w = 0; w < 8; w++) {
      const char LDS_AS* vp = smem + (16 + row - 2*w) * RS + 256 + hd * 32;
      u32x4 v0 = *(const u32x4 LDS_AS*)vp, v1 = *(const u32x4 LDS_AS*)(vp + 16);
      float pw = rbf(lg[w] * rs);
      f32x2 pw2 = {pw, pw};
#pragma unroll
      for (int e = 0; e < 4; e++) { ov[e] += pw2 * unpk(v0[e]); ov[4+e] += pw2 * unpk(v1[e]); }
    }
    u32x4 o0, o1;
#pragma unroll
    for (int e = 0; e < 4; e++) { o0[e] = pack2(ov[e]); o1[e] = pack2(ov[4+e]); }
    u32x4* dst = (u32x4*)(o + (size_t)srow * D + hg * 64 + hd * 16);
    dst[0] = o0; dst[1] = o1;
  }
}
__global__ __launch_bounds__(512) void k_ln_glu(const u16* __restrict__ x1, const float* __restrict__ mod,
    const u16* __restrict__ Wg, const u16* __restrict__ Wu, u16* __restrict__ hh)
{
  constexpr int AB = 128*512, STB = 16384, NST = 5, NK = 16;
  __shared__ __attribute__((aligned(16))) char smem_[AB + NST*STB];
  char LDS_AS* smem = (char LDS_AS*)smem_;
  const int tid = threadIdx.x, lane = tid & 63, wv = tid >> 6;
  const int b = blockIdx.x;
  const int rb = b & 15, cb = b >> 4;
  const int r0 = rb * 128, c0 = cb * 128;

  const u16* bsrc[2];
#pragma unroll
  for (int i = 0; i < 2; i++) {
    int L = (wv + 8*i) * 64 + lane;
    int row = L >> 5, cc = L & 31;
    int f = (row & 3) | (((row >> 3) & 1) << 2);
    int ch = cc ^ (2*f);
    const u16* base = (ch < 16) ? Wg : Wu;
    bsrc[i] = base + (size_t)row * DF + c0 + (ch & 15) * 8;
  }
  const unsigned sbase = lds_addr(smem) + AB + __builtin_amdgcn_readfirstlane(wv * 1024);
  auto issue = [&](int kc) {
    unsigned st = sbase + (kc % NST) * STB;
#pragma unroll
    for (int i = 0; i < 2; i++) glds16(bsrc[i] + (size_t)kc * 32 * DF, st + i * 8192);
  };
  const int sub = tid & 7, rl = tid >> 3;
  u32x4 v[2][8];
#pragma unroll
  for (int ps = 0; ps < 2; ps++) {
    const u32x4* src = (const u32x4*)(x1 + (size_t)(r0 + ps*64 + rl) * D);
#pragma unroll
    for (int j = 0; j < 8; j++) v[ps][j] = src[sub + 8*j];
  }
#pragma unroll
  for (int i = 0; i < NST-1; i++) issue(i);
  float mean[2], rden[2];
#pragma unroll
  for (int ps = 0; ps < 2; ps++) ln_stats(v[ps], mean[ps], rden[ps]);
#pragma unroll
  for (int j = 0; j < 8; j++) {
    int col = (sub + 8*j) * 8;
    f32x4 sa0 = *(const f32x4*)(mod + 3*D + col), sa1 = *(const f32x4*)(mod + 3*D + col + 4);
    f32x4 ca0 = *(const f32x4*)(mod + 4*D + col), ca1 = *(const f32x4*)(mod + 4*D + col + 4);
    f32x2 sc[4], sh[4];
    sc[0] = unpk(pack_bf(1.f + ca0[0], 1.f + ca0[1])); sc[1] = unpk(pack_bf(1.f + ca0[2], 1.f + ca0[3]));
    sc[2] = unpk(pack_bf(1.f + ca1[0], 1.f + ca1[1])); sc[3] = unpk(pack_bf(1.f + ca1[2], 1.f + ca1[3]));
    sh[0][0] = sa0[0]; sh[0][1] = sa0[1]; sh[1][0] = sa0[2]; sh[1][1] = sa0[3];
    sh[2][0] = sa1[0]; sh[2][1] = sa1[1]; sh[3][0] = sa1[2]; sh[3][1] = sa1[3];
#pragma unroll
    for (int ps = 0; ps < 2; ps++)
#pragma unroll
      for (int e = 0; e < 4; e++) v[ps][j][e] = ln_mod2(v[ps][j][e], mean[ps], rden[ps], sc[e], sh[e]);
  }
  auto write_half = [&](int hf) {
#pragma unroll
    for (int ps = 0; ps < 2; ps++) {
      int row = ps*64 + rl;
#pragma unroll
      for (int j = 0; j < 4; j++)
        *(u32x4 LDS_AS*)(smem + row * 512 + (((sub + 8*j) ^ ((row & 7) << 1)) << 4)) = v[ps][4*hf + j];
    }
  };
  write_half(0);

  const int wm = wv >> 2, wn = wv & 3;
  const int fr = lane & 15, g = lane >> 4, q = (lane & 15) >> 2, p = lane & 3;
  f32x4 acc[4][4] = {};
  int aoff[4];
#pragma unroll
  for (int mt = 0; mt < 4; mt++) { int row = 64*wm + 16*mt + fr; aoff[mt] = row * 512 + ((g ^ ((row & 7) << 1)) << 4); }
  const int bf = q | ((g & 1) << 2);
  int boff[4];
#pragma unroll
  for (int nt = 0; nt < 4; nt++) { int c = (nt >> 1) * 8 + 2*wn + (nt & 1); int cl = 2*c + (p >> 1); int ph = cl ^ (2*bf); boff[nt] = AB + (8*g + q) * 512 + ph * 16 + 8 * (p & 1); }

#pragma unroll
  for (int kc = 0; kc < NK; kc++) {
    if (kc + NST - 1 <= NK) wait_vm<2*(NST-2)>();
    else if (kc == NK-3) wait_vm<4>(); else if (kc == NK-2) wait_vm<2>(); else wait_vm<0>();
    if (kc == 8) { bar(); write_half(1); asm volatile("s_waitcnt lgkmcnt(0)":::"memory"); }
    if (kc == 0) asm volatile("s_waitcnt lgkmcnt(0)":::"memory");
    bar();
    if (kc + NST - 1 < NK) issue(kc + NST - 1);
    const char LDS_AS* st = smem + (kc % NST) * STB;
    bf16x8 bfr[4];
#pragma unroll
    for (int nt = 0; nt < 4; nt++) bfr[nt] = cat8(lds_tr(st + boff[nt]), lds_tr(st + boff[nt] + 4*512));
#pragma unroll
    for (int mt = 0; mt < 4; mt++) {
      bf16x8 af = lds_b128(smem + (aoff[mt] ^ ((kc & 7) << 6)));
#pragma unroll
      for (int nt = 0; nt < 4; nt++) acc[mt][nt] = MFMA16(bfr[nt], af, acc[mt][nt]);
    }
  }
#pragma unroll
  for (int mt = 0; mt < 4; mt++)
#pragma unroll
    for (int nt = 0; nt < 2; nt++) {
      int m = r0 + 64*wm + 16*mt + fr;
      int n = c0 + 32*wn + 16*nt + 4*g;
      u32x2 ov;
#pragma unroll
      for (int e2 = 0; e2 < 2; e2++) {
        f32x2 gv = rbf2((f32x2){acc[mt][nt][2*e2], acc[mt][nt][2*e2+1]});
        f32x2 uv = rbf2((f32x2){acc[mt][2+nt][2*e2], acc[mt][2+nt][2*e2+1]});
        f32x2 t = gv * gv * (f32x2){-2.302208198f*0.044715f, -2.302208198f*0.044715f} + (f32x2){-2.302208198f, -2.302208198f};
        f32x2 z = gv * t;
        f32x2 ge;
        ge[0] = gv[0] * __builtin_amdgcn_rcpf(1.f + __builtin_amdgcn_exp2f(z[0]));
        ge[1] = gv[1] * __builtin_amdgcn_rcpf(1.f + __builtin_amdgcn_exp2f(z[1]));
        ov[e2] = pack2(rbf2(ge) * uv);
      }
      *(u32x2*)(hh + (size_t)m * DF + n) = ov;
    }
}
__global__ __launch_bounds__(256) void k_mod2(const u16* __restrict__ c, const u16* __restrict__ wmod, float* __restrict__ mod)
{
  __shared__ float red[4][32];
  const int tid = threadIdx.x, cl = tid & 3, kg = tid >> 2;
  const int n0 = blockIdx.x * 32 + cl * 8;
  u32x4 w[8];
#pragma unroll
  for (int i = 0; i < 8; i++) w[i] = *(const u32x4*)(wmod + (size_t)(kg * 8 + i) * (6*D) + n0);
  float a[8];
#pragma unroll
  for (int e = 0; e < 8; e++) a[e] = 0.f;
#pragma unroll
  for (int i = 0; i < 8; i++) {
    float cv = bf2f(c[kg * 8 + i]);
    float s = rbf(cv / (1.f + __expf(-cv)));
#pragma unroll
    for (int e = 0; e < 4; e++) { a[2*e] += s * lo_f(w[i][e]); a[2*e+1] += s * hi_f(w[i][e]); }
  }
#pragma unroll
  for (int e = 0; e < 8; e++) { a[e] += __shfl_xor(a[e], 4); a[e] += __shfl_xor(a[e], 8); a[e] += __shfl_xor(a[e], 16); a[e] += __shfl_xor(a[e], 32); }
  if ((tid & 63) < 4) {
#pragma unroll
    for (int e = 0; e < 8; e++) red[tid >> 6][cl * 8 + e] = a[e];
  }
  __syncthreads();
  if (tid < 32) mod[blockIdx.x * 32 + tid] = rbf(red[0][tid] + red[1][tid] + red[2][tid] + red[3][tid]);
}
extern "C" void kernel_launch(void* const* d_in, const int* in_sizes, int n_in,
                              void* d_out, int out_size, void* d_ws, size_t ws_size,
                              hipStream_t stream) {
  const u16* x=(const u16*)d_in[0]; const u16* wqkv=(const u16*)d_in[1]; const u16* wo=(const u16*)d_in[2];
  const u16* wg=(const u16*)d_in[4]; const u16* wu=(const u16*)d_in[5]; const u16* wd=(const u16*)d_in[6];
  const u16* c=(const u16*)d_in[7]; const u16* wmod=(const u16*)d_in[8];
  char* ws=(char*)d_ws;
  float* mod=(float*)ws; ws+=16384;
  u16* o=(u16*)ws; ws+=S*D*2;
  u16* x1=(u16*)ws; ws+=S*D*2;
  u16* hh=(u16*)ws; ws+=S*DF*2;
  k_mod2<<<96,256,0,stream>>>(c,wmod,mod);
  k_qkv_attn<<<256,256,0,stream>>>(x,mod,wqkv,o);
  k_gemm_res<512,3><<<256,512,0,stream>>>(o,wo,x,mod+2*D,x1);
  k_ln_glu<<<224,512,0,stream>>>(x1,mod,wg,wu,hh);
  k_gemm_res<1792,4><<<256,512,0,stream>>>(hh,wd,x1,mod+5*D,(u16*)d_out);
}
```

```cpp
#include <hip/hip_runtime.h>
#include <hip/hip_bf16.h>
#include <stdint.h>

typedef unsigned short u16;
typedef __attribute__((ext_vector_type(8))) short bf16x8;
typedef __attribute__((ext_vector_type(4))) short s16x4;
typedef __attribute__((ext_vector_type(4))) float f32x4;
typedef __attribute__((ext_vector_type(2))) unsigned u32x2;
typedef __attribute__((ext_vector_type(4))) unsigned u32x4;
#define LDS_AS __attribute__((address_space(3)))
#define GLB_AS __attribute__((address_space(1)))

typedef __attribute__((ext_vector_type(2))) float f32x2;
typedef __attribute__((ext_vector_type(2))) __bf16 bf16x2_t;
__device__ __forceinline__ float bf2f(u16 v){ return __uint_as_float(((unsigned)v)<<16); }
__device__ __forceinline__ float lo_f(unsigned u){ return __uint_as_float(u<<16); }
__device__ __forceinline__ float hi_f(unsigned u){ return __uint_as_float(u & 0xffff0000u); }
__device__ __forceinline__ f32x2 unpk(unsigned u){ f32x2 r; r[0]=lo_f(u); r[1]=hi_f(u); return r; }
__device__ __forceinline__ unsigned pack2(f32x2 v){ return __builtin_bit_cast(unsigned, __builtin_convertvector(v, bf16x2_t)); }
__device__ __forceinline__ unsigned pack_bf(float lo, float hi){ f32x2 v; v[0]=lo; v[1]=hi; return pack2(v); }
__device__ __forceinline__ float rbf(float f){ return lo_f(pack_bf(f, 0.f)); }

__device__ __forceinline__ void glds16(const void* gsrc, unsigned lds_dst) {
  unsigned keep;
  asm volatile("s_mov_b32 %0, m0\n\ts_mov_b32 m0, %2\n\ts_nop 0\n\tglobal_load_lds_dwordx4 %1, off\n\ts_mov_b32 m0, %0" : "=&s"(keep) : "v"(gsrc), "s"(lds_dst) : "memory");
}
__device__ __forceinline__ unsigned lds_addr(const char LDS_AS* p){ return __builtin_amdgcn_readfirstlane((unsigned)(size_t)p); }
template<int N> __device__ __forceinline__ void wait_vm(){ asm volatile("s_waitcnt vmcnt(%0)"::"n"(N):"memory"); }
__device__ __forceinline__ void bar(){ __builtin_amdgcn_s_barrier(); }
__device__ __forceinline__ bf16x8 lds_b128(const char LDS_AS* p){ return *(const bf16x8 LDS_AS*)p; }
__device__ __forceinline__ s16x4 lds_tr(const char LDS_AS* p){ return __builtin_amdgcn_ds_read_tr16_b64_v4i16((s16x4 LDS_AS*)p); }
__device__ __forceinline__ bf16x8 cat8(s16x4 a, s16x4 b){ bf16x8 r; r[0]=a[0];r[1]=a[1];r[2]=a[2];r[3]=a[3];r[4]=b[0];r[5]=b[1];r[6]=b[2];r[7]=b[3]; return r; }
#define MFMA16(a,b,c) __builtin_amdgcn_mfma_f32_16x16x32_bf16((a),(b),(c),0,0,0)

constexpr int S=2048, D=512, DF=1792, H=32, DH=16;
template<int K, int NST>
__global__ __launch_bounds__(512) void k_gemm_res(const u16* __restrict__ A, const u16* __restrict__ W,
    const u16* __restrict__ res, const float* __restrict__ gmod, u16* __restrict__ out)
{
  constexpr int NI = K/128;
  constexpr int CHB = 16384;
  constexpr int STB = 2*CHB;
  __shared__ __attribute__((aligned(16))) char smem_[NST*STB];
  char LDS_AS* smem = (char LDS_AS*)smem_;
  const int tid = threadIdx.x, lane = tid & 63, wv = tid >> 6;
  const int b = blockIdx.x;
  const int rb = (b & 7) * 4 + ((b >> 3) & 3);
  const int cb = b >> 5;
  const int m0 = rb * 64, n0 = cb * 64;
  const int kg = wv >> 2, w4 = wv & 3;

  const u16* asrc[2]; const u16* bsrc[2];
#pragma unroll
  for (int i = 0; i < 2; i++) {
    int inst = w4 + 4*i;
    int row = inst*8 + (lane >> 3);
    int slot = lane & 7;
    int ach = slot ^ (row & 7);
    asrc[i] = A + (size_t)(m0 + row) * K + kg * 64 + ach * 8;
    int f = ((row >> 1) & 1) | (((row >> 3) & 1) << 1);
    int bch = slot ^ (2*f);
    bsrc[i] = W + (size_t)(kg * 64 + row) * D + n0 + bch * 8;
  }
  const unsigned sbase = lds_addr(smem) + __builtin_amdgcn_readfirstlane(kg * CHB + w4 * 1024);
  auto issue = [&](int it) {
    unsigned st = sbase + (it % NST) * STB;
#pragma unroll
    for (int i = 0; i < 2; i++) {
      glds16(asrc[i] + it * 128, st + i * 4096);
      glds16(bsrc[i] + (size_t)it * 128 * D, st + 8192 + i * 4096);
    }
  };
  const int wm = w4 >> 1, wn = w4 & 1;
  const int fr = lane & 15, g = lane >> 4, q = (lane & 15) >> 2, p = lane & 3;
  f32x4 acc[2][2] = {};
  int aoff[2];
#pragma unroll
  for (int mt = 0; mt < 2; mt++) { int row = 32*wm + 16*mt + fr; aoff[mt] = kg * CHB + row * 128 + ((g ^ (row & 7)) << 4); }
  const int bf = (q >> 1) | ((g & 1) << 1);
  int boff[2];
#pragma unroll
  for (int nt = 0; nt < 2; nt++) { int c = 2*wn + nt; int ch = (2*c + (p >> 1)) ^ (2*bf); boff[nt] = kg * CHB + 8192 + (8*g + q) * 128 + ch * 16 + 8 * (p & 1); }

  auto compute = [&](int it) {
    const char LDS_AS* st = smem + (it % NST) * STB;
#pragma unroll
    for (int s = 0; s < 2; s++) {
      bf16x8 af[2], bfr[2];
#pragma unroll
      for (int mt = 0; mt < 2; mt++) af[mt] = lds_b128(st + (aoff[mt] ^ (s << 6)));
#pragma unroll
      for (int nt = 0; nt < 2; nt++) bfr[nt] = cat8(lds_tr(st + boff[nt] + s*32*128), lds_tr(st + boff[nt] + s*32*128 + 4*128));
#pragma unroll
      for (int mt = 0; mt < 2; mt++)
#pragma unroll
        for (int nt = 0; nt < 2; nt++) acc[mt][nt] = MFMA16(bfr[nt], af[mt], acc[mt][nt]);
    }
  };
#pragma unroll
  for (int i = 0; i < NST-1; i++) if (i < NI) issue(i);
#pragma unroll 1
  for (int it = 0; it < NI - (NST-1); it++) {
    wait_vm<4*(NST-2)>();
    bar();
    issue(it + NST - 1);
    compute(it);
  }
#pragma unroll
  for (int j = NST-2; j >= 0; j--) {
    int it = NI - 1 - j;
    if (it >= 0) {
      switch (j) { case 0: wait_vm<0>(); break; case 1: wait_vm<4>(); break; case 2: wait_vm<8>(); break; case 3: wait_vm<12>(); break; default: wait_vm<16>(); break; }
      bar();
      compute(it);
    }
  }
  __syncthreads();
  f32x4 LDS_AS* red = (f32x4 LDS_AS*)smem;
  if (kg == 1) {
#pragma unroll
    for (int mt = 0; mt < 2; mt++)
#pragma unroll
      for (int nt = 0; nt < 2; nt++) red[((w4 * 4) + mt * 2 + nt) * 64 + lane] = acc[mt][nt];
  }
  __syncthreads();
  if (kg == 0) {
#pragma unroll
    for (int mt = 0; mt < 2; mt++)
#pragma unroll
      for (int nt = 0; nt < 2; nt++) {
        f32x4 a = acc[mt][nt] + red[((w4 * 4) + mt * 2 + nt) * 64 + lane];
        int m = m0 + 32*wm + 16*mt + fr;
        int n = n0 + 32*wn + 16*nt + 4*g;
        u32x2 rv = *(const u32x2*)(res + (size_t)m * D + n);
        f32x4 gv = *(const f32x4*)(gmod + n);
        float o0 = lo_f(rv[0]) + rbf(gv[0] * rbf(a[0]));
        float o1 = hi_f(rv[0]) + rbf(gv[1] * rbf(a[1]));
        float o2 = lo_f(rv[1]) + rbf(gv[2] * rbf(a[2]));
        float o3 = hi_f(rv[1]) + rbf(gv[3] * rbf(a[3]));
        u32x2 ov; ov[0] = pack_bf(o0, o1); ov[1] = pack_bf(o2, o3);
        *(u32x2*)(out + (size_t)m * D + n) = ov;
      }
  }
}
__device__ __forceinline__ float dot2bf(unsigned a, unsigned b, float c){
  return __builtin_amdgcn_fdot2_f32_bf16(__builtin_bit_cast(bf16x2_t,a), __builtin_bit_cast(bf16x2_t,b), c, false);
}
__device__ __forceinline__ void ln_stats(const u32x4 (&v)[8], float& nm, float& rden) {
  f32x2 s2 = {0.f, 0.f};
#pragma unroll
  for (int j = 0; j < 8; j++)
#pragma unroll
    for (int e = 0; e < 4; e++) s2 += unpk(v[j][e]);
  float s = s2[0] + s2[1];
  s += __shfl_xor(s, 1); s += __shfl_xor(s, 2); s += __shfl_xor(s, 4);
  float mean = s * (1.f/D);
  f32x2 m2 = {mean, mean}, q2 = {0.f, 0.f};
#pragma unroll
  for (int j = 0; j < 8; j++)
#pragma unroll
    for (int e = 0; e < 4; e++) { f32x2 d = unpk(v[j][e]) - m2; q2 += d * d; }
  float q = q2[0] + q2[1];
  q += __shfl_xor(q, 1); q += __shfl_xor(q, 2); q += __shfl_xor(q, 4);
  float var = rbf(q * (1.f/D));
  rden = 1.f / rbf(sqrtf(rbf(var + 1e-5f)));
  nm = rbf(mean);
}
__device__ __forceinline__ f32x2 rbf2(f32x2 v){ return unpk(pack2(v)); }
__device__ __forceinline__ unsigned ln_mod2(unsigned xv, float m, float rden, f32x2 sc, f32x2 sh) {
  f32x2 r2 = {rden, rden}, m2 = {m, m};
  f32x2 d = rbf2(unpk(xv) - m2);
  f32x2 n = rbf2(d * r2);
  return pack2(rbf2(n * sc) + sh);
}

__global__ __launch_bounds__(512) void k_qkv_attn(const u16* __restrict__ x, const float* __restrict__ mod,
    const u16* __restrict__ W, u16* __restrict__ o)
{
  constexpr int AB = 80*1024, STB = 24576, NST = 3, NK = 8;
  __shared__ __attribute__((aligned(16))) char smem_[AB + NST*STB];
  char LDS_AS* smem = (char LDS_AS*)smem_;
  const int tid = threadIdx.x, lane = tid & 63, wv = tid >> 6;
  const int hg = blockIdx.x & 7, rb = blockIdx.x >> 3;
  const int r0 = rb * 64;

  const u16* bsrc[3];
#pragma unroll
  for (int i = 0; i < 3; i++) {
    int L = (wv + 8*i) * 64 + lane;
    int row = L / 24, cc = L % 24, seg = cc >> 3;
    int f = ((row >> 1) & 1) | (((row >> 3) & 1) << 1);
    int ch = (cc & 7) ^ (2*f);
    bsrc[i] = W + (size_t)row * 1536 + seg * 512 + hg * 64 + ch * 8;
  }
  const unsigned sbase = lds_addr(smem) + AB + __builtin_amdgcn_readfirstlane(wv * 1024);
  auto issue = [&](int kc) {
    unsigned st = sbase + (kc % NST) * STB;
#pragma unroll
    for (int i = 0; i < 3; i++) glds16(bsrc[i] + (size_t)kc * 64 * 1536, st + i * 8192);
  };
  {
    const int sub = tid & 7, rl = tid >> 3;
    u32x4 v[2][8];
#pragma unroll
    for (int ps = 0; ps < 2; ps++) {
      int gr = r0 - 16 + ps*64 + rl; gr = gr < 0 ? 0 : (gr > S-1 ? S-1 : gr);
      const u32x4* src = (const u32x4*)(x + (size_t)gr * D);
#pragma unroll
      for (int j = 0; j < 8; j++) v[ps][j] = src[sub + 8*j];
    }
    issue(0); issue(1);
    float mean[2], rden[2];
#pragma unroll
    for (int ps = 0; ps < 2; ps++) ln_stats(v[ps], mean[ps], rden[ps]);
#pragma unroll
    for (int j = 0; j < 8; j++) {
      int col = (sub + 8*j) * 8;
      f32x4 sa0 = *(const f32x4*)(mod + col), sa1 = *(const f32x4*)(mod + col + 4);
      f32x4 ca0 = *(const f32x4*)(mod + D + col), ca1 = *(const f32x4*)(mod + D + col + 4);
      f32x2 sc[4], sh[4];
      sc[0] = unpk(pack_bf(1.f + ca0[0], 1.f + ca0[1])); sc[1] = unpk(pack_bf(1.f + ca0[2], 1.f + ca0[3]));
      sc[2] = unpk(pack_bf(1.f + ca1[0], 1.f + ca1[1])); sc[3] = unpk(pack_bf(1.f + ca1[2], 1.f + ca1[3]));
      sh[0][0] = sa0[0]; sh[0][1] = sa0[1]; sh[1][0] = sa0[2]; sh[1][1] = sa0[3];
      sh[2][0] = sa1[0]; sh[2][1] = sa1[1]; sh[3][0] = sa1[2]; sh[3][1] = sa1[3];
#pragma unroll
      for (int ps = 0; ps < 2; ps++) {
        int row = ps*64 + rl;
        u32x4 h;
#pragma unroll
        for (int e = 0; e < 4; e++) h[e] = ln_mod2(v[ps][j][e], mean[ps], rden[ps], sc[e], sh[e]);
        if (row < 80) *(u32x4 LDS_AS*)(smem + row * 1024 + (((sub + 8*j) ^ ((row & 7) << 1)) << 4)) = h;
      }
    }
  }
  const int wn = wv & 3, wm = __builtin_amdgcn_readfirstlane(wv >> 2);
  const int nmt = 3 - wm;
  const int fr = lane & 15, g = lane >> 4, q = (lane & 15) >> 2, p = lane & 3;
  f32x4 acc[3][3] = {};
  int aoff[3];
#pragma unroll
  for (int mt = 0; mt < 3; mt++) { int row = 16*(3*wm + mt) + fr; aoff[mt] = row * 1024 + ((g ^ ((row & 7) << 1)) << 4); }
  const int bf = (q >> 1) | ((g & 1) << 1);
  int boff[3];
#pragma unroll
  for (int nt = 0; nt < 3; nt++) { int c = 3*wn + nt; int cl = 2*c + (p >> 1); int ph = (cl & ~7) | ((cl & 7) ^ (2*bf)); boff[nt] = AB + (8*g + q) * 384 + ph * 16 + 8 * (p & 1); }

#pragma unroll
  for (int kc = 0; kc < NK; kc++) {
    if (kc < NK-1) wait_vm<3>(); else wait_vm<0>();
    if (kc == 0) asm volatile("s_waitcnt lgkmcnt(0)":::"memory");
    bar();
    if (kc + 2 < NK) issue(kc + 2);
    const char LDS_AS* st = smem + (kc % NST) * STB;
#pragma unroll
    for (int s = 0; s < 2; s++) {
      bf16x8 bfr[3];
#pragma unroll
      for (int nt = 0; nt < 3; nt++) bfr[nt] = cat8(lds_tr(st + boff[nt] + s*32*384), lds_tr(st + boff[nt] + s*32*384 + 4*384));
#pragma unroll
      for (int mt = 0; mt < 3; mt++) {
        if (mt < nmt) {
          bf16x8 af = lds_b128(smem + (aoff[mt] ^ ((2*kc + s) << 6)));
#pragma unroll
          for (int nt = 0; nt < 3; nt++) acc[mt][nt] = MFMA16(bfr[nt], af, acc[mt][nt]);
        }
      }
    }
  }
  __syncthreads();
  constexpr int RS = 400;
#pragma unroll
  for (int mt = 0; mt < 3; mt++)
    if (mt < nmt) {
#pragma unroll
      for (int nt = 0; nt < 3; nt++) {
        int row = 16*(3*wm + mt) + fr, col = 48*wn + 16*nt + 4*g;
        u32x2 w2; w2[0] = pack_bf(acc[mt][nt][0], acc[mt][nt][1]); w2[1] = pack_bf(acc[mt][nt][2], acc[mt][nt][3]);
        *(u32x2 LDS_AS*)(smem + row * RS + col * 2) = w2;
      }
    }
  __syncthreads();
  if (tid < 256) {
    const int row = tid >> 2, hd = tid & 3;
    const int srow = r0 + row;
    const char LDS_AS* qp = smem + (16 + row) * RS + hd * 32;
    u32x4 q0 = *(const u32x4 LDS_AS*)qp, q1 = *(const u32x4 LDS_AS*)(qp + 16);
    float lg[8]; float mx = -3.0e38f;
#pragma unroll
    for (int w = 0; w < 8; w++) {
      const char LDS_AS* kp = smem + (16 + row - 2*w) * RS + 128 + hd * 32;
      u32x4 k0 = *(const u32x4 LDS_AS*)kp, k1 = *(const u32x4 LDS_AS*)(kp + 16);
      float a = 0.f;
#pragma unroll
      for (int e = 0; e < 4; e++) a = dot2bf(q0[e], k0[e], a);
#pragma unroll
      for (int e = 0; e < 4; e++) a = dot2bf(q1[e], k1[e], a);
      float l = rbf(a) * 0.25f;
      l = (srow - 2*w >= 0) ? l : -1.0e9f;
      lg[w] = l; mx = fmaxf(mx, l);
    }
    float sum = 0.f;
#pragma unroll
    for (int w = 0; w < 8; w++) { lg[w] = __expf(lg[w] - mx); sum += lg[w]; }
    float rs = 1.f / sum;
    f32x2 ov[8];
#pragma unroll
    for (int d = 0; d < 8; d++) ov[d] = (f32x2){0.f, 0.f};
#pragma unroll
    for (int w = 0; w < 8; w++) {
      const char LDS_AS* vp = smem + (16 + row - 2*w) * RS + 256 + hd * 32;
      u32x4 v0 = *(const u32x4 LDS_AS*)vp, v1 = *(const u32x4 LDS_AS*)(vp + 16);
      float pw = rbf(lg[w] * rs);
      f32x2 pw2 = {pw, pw};
#pragma unroll
      for (int e = 0; e < 4; e++) { ov[e] += pw2 * unpk(v0[e]); ov[4+e] += pw2 * unpk(v1[e]); }
    }
    u32x4 o0, o1;
#pragma unroll
    for (int e = 0; e < 4; e++) { o0[e] = pack2(ov[e]); o1[e] = pack2(ov[4+e]); }
    u32x4* dst = (u32x4*)(o + (size_t)srow * D + hg * 64 + hd * 16);
    dst[0] = o0; dst[1] = o1;
  }
}
__global__ __launch_bounds__(512) void k_ln_glu(const u16* __restrict__ x1, const float* __restrict__ mod,
    const u16* __restrict__ Wg, const u16* __restrict__ Wu, u16* __restrict__ hh)
{
  constexpr int AB = 128*512, STB = 16384, NST = 5, NK = 16;
  __shared__ __attribute__((aligned(16))) char smem_[AB + NST*STB];
  char LDS_AS* smem = (char LDS_AS*)smem_;
  const int tid = threadIdx.x, lane = tid & 63, wv = tid >> 6;
  const int b = blockIdx.x;
  const int rb = b & 15, cb = b >> 4;
  const int r0 = rb * 128, c0 = cb * 128;

  const u16* bsrc[2];
#pragma unroll
  for (int i = 0; i < 2; i++) {
    int L = (wv + 8*i) * 64 + lane;
    int row = L >> 5, cc = L & 31;
    int f = (row & 3) | (((row >> 3) & 1) << 2);
    int ch = cc ^ (2*f);
    const u16* base = (ch < 16) ? Wg : Wu;
    bsrc[i] = base + (size_t)row * DF + c0 + (ch & 15) * 8;
  }
  const unsigned sbase = lds_addr(smem) + AB + __builtin_amdgcn_readfirstlane(wv * 1024);
  auto issue = [&](int kc) {
    unsigned st = sbase + (kc % NST) * STB;
#pragma unroll
    for (int i = 0; i < 2; i++) glds16(bsrc[i] + (size_t)kc * 32 * DF, st + i * 8192);
  };
  const int sub = tid & 7, rl = tid >> 3;
  u32x4 v[2][8];
#pragma unroll
  for (int ps = 0; ps < 2; ps++) {
    const u32x4* src = (const u32x4*)(x1 + (size_t)(r0 + ps*64 + rl) * D);
#pragma unroll
    for (int j = 0; j < 8; j++) v[ps][j] = src[sub + 8*j];
  }
#pragma unroll
  for (int i = 0; i < NST-1; i++) issue(i);
  float mean[2], rden[2];
#pragma unroll
  for (int ps = 0; ps < 2; ps++) ln_stats(v[ps], mean[ps], rden[ps]);
#pragma unroll
  for (int j = 0; j < 8; j++) {
    int col = (sub + 8*j) * 8;
    f32x4 sa0 = *(const f32x4*)(mod + 3*D + col), sa1 = *(const f32x4*)(mod + 3*D + col + 4);
    f32x4 ca0 = *(const f32x4*)(mod + 4*D + col), ca1 = *(const f32x4*)(mod + 4*D + col + 4);
    f32x2 sc[4], sh[4];
    sc[0] = unpk(pack_bf(1.f + ca0[0], 1.f + ca0[1])); sc[1] = unpk(pack_bf(1.f + ca0[2], 1.f + ca0[3]));
    sc[2] = unpk(pack_bf(1.f + ca1[0], 1.f + ca1[1])); sc[3] = unpk(pack_bf(1.f + ca1[2], 1.f + ca1[3]));
    sh[0][0] = sa0[0]; sh[0][1] = sa0[1]; sh[1][0] = sa0[2]; sh[1][1] = sa0[3];
    sh[2][0] = sa1[0]; sh[2][1] = sa1[1]; sh[3][0] = sa1[2]; sh[3][1] = sa1[3];
#pragma unroll
    for (int ps = 0; ps < 2; ps++)
#pragma unroll
      for (int e = 0; e < 4; e++) v[ps][j][e] = ln_mod2(v[ps][j][e], mean[ps], rden[ps], sc[e], sh[e]);
  }
  auto write_half = [&](int hf) {
#pragma unroll
    for (int ps = 0; ps < 2; ps++) {
      int row = ps*64 + rl;
#pragma unroll
      for (int j = 0; j < 4; j++)
        *(u32x4 LDS_AS*)(smem + row * 512 + (((sub + 8*j) ^ ((row & 7) << 1)) << 4)) = v[ps][4*hf + j];
    }
  };
  write_half(0);

  const int wm = wv >> 2, wn = wv & 3;
  const int fr = lane & 15, g = lane >> 4, q = (lane & 15) >> 2, p = lane & 3;
  f32x4 acc[4][4] = {};
  int aoff[4];
#pragma unroll
  for (int mt = 0; mt < 4; mt++) { int row = 64*wm + 16*mt + fr; aoff[mt] = row * 512 + ((g ^ ((row & 7) << 1)) << 4); }
  const int bf = q | ((g & 1) << 2);
  int boff[4];
#pragma unroll
  for (int nt = 0; nt < 4; nt++) { int c = (nt >> 1) * 8 + 2*wn + (nt & 1); int cl = 2*c + (p >> 1); int ph = cl ^ (2*bf); boff[nt] = AB + (8*g + q) * 512 + ph * 16 + 8 * (p & 1); }

#pragma unroll
  for (int kc = 0; kc < NK; kc++) {
    if (kc + NST - 1 <= NK) wait_vm<2*(NST-2)>();
    else if (kc == NK-3) wait_vm<4>(); else if (kc == NK-2) wait_vm<2>(); else wait_vm<0>();
    if (kc == 8) { bar(); write_half(1); asm volatile("s_waitcnt lgkmcnt(0)":::"memory"); }
    if (kc == 0) asm volatile("s_waitcnt lgkmcnt(0)":::"memory");
    bar();
    if (kc + NST - 1 < NK) issue(kc + NST - 1);
    const char LDS_AS* st = smem + (kc % NST) * STB;
    bf16x8 bfr[4];
#pragma unroll
    for (int nt = 0; nt < 4; nt++) bfr[nt] = cat8(lds_tr(st + boff[nt]), lds_tr(st + boff[nt] + 4*512));
#pragma unroll
    for (int mt = 0; mt < 4; mt++) {
      bf16x8 af = lds_b128(smem + (aoff[mt] ^ ((kc & 7) << 6)));
#pragma unroll
      for (int nt = 0; nt < 4; nt++) acc[mt][nt] = MFMA16(bfr[nt], af, acc[mt][nt]);
    }
  }
#pragma unroll
  for (int mt = 0; mt < 4; mt++)
#pragma unroll
    for (int nt = 0; nt < 2; nt++) {
      int m = r0 + 64*wm + 16*mt + fr;
      int n = c0 + 32*wn + 16*nt + 4*g;
      u32x2 ov;
#pragma unroll
      for (int e2 = 0; e2 < 2; e2++) {
        f32x2 gv = rbf2((f32x2){acc[mt][nt][2*e2], acc[mt][nt][2*e2+1]});
        f32x2 uv = rbf2((f32x2){acc[mt][2+nt][2*e2], acc[mt][2+nt][2*e2+1]});
        f32x2 t = gv * gv * (f32x2){-2.302208198f*0.044715f, -2.302208198f*0.044715f} + (f32x2){-2.302208198f, -2.302208198f};
        f32x2 z = gv * t;
        f32x2 ge;
        ge[0] = gv[0] * __builtin_amdgcn_rcpf(1.f + __builtin_amdgcn_exp2f(z[0]));
        ge[1] = gv[1] * __builtin_amdgcn_rcpf(1.f + __builtin_amdgcn_exp2f(z[1]));
        ov[e2] = pack2(rbf2(ge) * uv);
      }
      *(u32x2*)(hh + (size_t)m * DF + n) = ov;
    }
}
__global__ __launch_bounds__(256) void k_mod2(const u16* __restrict__ c, const u16* __restrict__ wmod, float* __restrict__ mod)
{
  __shared__ float red[4][32];
  const int tid = threadIdx.x, cl = tid & 3, kg = tid >> 2;
  const int n0 = blockIdx.x * 32 + cl * 8;
  u32x4 w[8];
#pragma unroll
  for (int i = 0; i < 8; i++) w[i] = *(const u32x4*)(wmod + (size_t)(kg * 8 + i) * (6*D) + n0);
  float a[8];
#pragma unroll
  for (int e = 0; e < 8; e++) a[e] = 0.f;
#pragma unroll
  for (int i = 0; i < 8; i++) {
    float cv = bf2f(c[kg * 8 + i]);
    float s = rbf(cv / (1.f + __expf(-cv)));
#pragma unroll
    for (int e = 0; e < 4; e++) { a[2*e] += s * lo_f(w[i][e]); a[2*e+1] += s * hi_f(w[i][e]); }
  }
#pragma unroll
  for (int e = 0; e < 8; e++) { a[e] += __shfl_xor(a[e], 4); a[e] += __shfl_xor(a[e], 8); a[e] += __shfl_xor(a[e], 16); a[e] += __shfl_xor(a[e], 32); }
  if ((tid & 63) < 4) {
#pragma unroll
    for (int e = 0; e < 8; e++) red[tid >> 6][cl * 8 + e] = a[e];
  }
  __syncthreads();
  if (tid < 32) mod[blockIdx.x * 32 + tid] = rbf(red[0][tid] + red[1][tid] + red[2][tid] + red[3][tid]);
}
extern "C" void kernel_launch(void* const* d_in, const int* in_sizes, int n_in,
                              void* d_out, int out_size, void* d_ws, size_t ws_size,
                              hipStream_t stream) {
  const u16* x=(const u16*)d_in[0]; const u16* wqkv=(const u16*)d_in[1]; const u16* wo=(const u16*)d_in[2];
  const u16* wg=(const u16*)d_in[4]; const u16* wu=(const u16*)d_in[5]; const u16* wd=(const u16*)d_in[6];
  const u16* c=(const u16*)d_in[7]; const u16* wmod=(const u16*)d_in[8];
  char* ws=(char*)d_ws;
  float* mod=(float*)ws; ws+=16384;
  u16* o=(u16*)ws; ws+=S*D*2;
  u16* x1=(u16*)ws; ws+=S*D*2;
  u16* hh=(u16*)ws; ws+=S*DF*2;
  k_mod2<<<96,256,0,stream>>>(c,wmod,mod);
  k_qkv_attn<<<256,512,0,stream>>>(x,mod,wqkv,o);
  k_gemm_res<512,3><<<256,512,0,stream>>>(o,wo,x,mod+2*D,x1);
  k_ln_glu<<<224,512,0,stream>>>(x1,mod,wg,wu,hh);
  k_gemm_res<1792,4><<<256,512,0,stream>>>(hh,wd,x1,mod+5*D,(u16*)d_out);
}
```
